```python
import torch

def _softmax(x, axis=-1):
    return torch.softmax(x, dim=axis)

def _gelu(x):
    return torch.nn.functional.gelu(x, approximate='tanh')

def _silu(x):
    return torch.nn.functional.silu(x)

def _sigmoid(x):
    return torch.sigmoid(x)

def _layernorm(x, axis=-1, eps=1e-05):
    m = x.mean(dim=axis, keepdim=True)
    v = x.var(dim=axis, keepdim=True, unbiased=False)
    return (x - m) / torch.sqrt(v + eps)

def _scatter_add(tgt, idx, src, axis=0):
    return tgt.clone().index_add_(axis, idx, src)

def _segment_sum(src, seg, n):
    out = torch.zeros((n,) + src.shape[1:], dtype=src.dtype)
    return out.index_add_(0, seg, src)

def _pad(x, pw):
    flat = tuple((v for pair in reversed(pw) for v in pair))
    return torch.nn.functional.pad(x, flat)
kernel_name = 'adaLN_cond__ssm_conv_s4__glu_gated_glu__prompt'

def setup_inputs():

    _w = lambda a: (lambda t: t.to(torch.bfloat16) if t.is_floating_point() else t)(torch.as_tensor(a))

    def _A_setup():
        B, L, D, N = (1, 2048, 448, 256)
        A_re = -(lambda _x: torch.exp(torch.as_tensor(_x)))((lambda *s: torch.randn(*s, dtype=torch.bfloat16) * 3.2)(D, N))
        A_im = torch.pi * (lambda *s: torch.randn(*s, dtype=torch.bfloat16) * 0.2)(D, N)
        B_re = (lambda *s: torch.randn(*s, dtype=torch.bfloat16) * 0.2)(D, N)
        B_im = (lambda *s: torch.randn(*s, dtype=torch.bfloat16) * 0.2)(D, N)
        C_re = (lambda *s: torch.randn(*s, dtype=torch.bfloat16) * 0.2)(D, N)
        C_im = (lambda *s: torch.randn(*s, dtype=torch.bfloat16) * 0.2)(D, N)
        log_dt = (lambda *s: torch.randn(*s, dtype=torch.bfloat16) * 0.2)(D) * 0.5 - 4.0
        Dskip = (lambda *s: torch.randn(*s, dtype=torch.bfloat16) * 0.2)(D)
        return {'u': _w((lambda *s: torch.randn(*s, dtype=torch.bfloat16) * 0.05)(B, L, D)), 'A_re': _w(A_re), 'A_im': _w(A_im), 'B_re': _w(B_re), 'B_im': _w(B_im), 'C_re': _w(C_re), 'C_im': _w(C_im), 'log_dt': _w(log_dt), 'Dskip': _w(Dskip)}
    _A_in = _A_setup()

    def _B_setup():
        B, S, D, F = (1, 2048, 448, 1792)
        return {'x': _w((lambda *s: torch.randn(*s, dtype=torch.bfloat16) * 0.05)(B, S, D)), 'w_gate': _w((lambda *s: torch.randn(*s, dtype=torch.bfloat16) * 0.4)(D, F)), 'w_up': _w((lambda *s: torch.randn(*s, dtype=torch.bfloat16) * 0.2)(D, F)), 'w_down': _w((lambda *s: torch.randn(*s, dtype=torch.bfloat16) * 0.2)(F, D))}
    _B_in = _B_setup()
    return {'A_u': _A_in['u'], 'A_A_re': _A_in['A_re'], 'A_A_im': _A_in['A_im'], 'A_B_re': _A_in['B_re'], 'A_B_im': _A_in['B_im'], 'A_C_re': _A_in['C_re'], 'A_C_im': _A_in['C_im'], 'A_log_dt': _A_in['log_dt'], 'A_Dskip': _A_in['Dskip'], 'B_x': _B_in['x'], 'B_w_gate': _B_in['w_gate'], 'B_w_up': _B_in['w_up'], 'B_w_down': _B_in['w_down'], 'c': _w((lambda *s: torch.randn(*s, dtype=torch.bfloat16) * 0.4)(1, 448) * 8.0), 'w_mod': _w((lambda *s: torch.randn(*s, dtype=torch.bfloat16) * 0.4)(448, 6 * 448) * 448 ** (-0.5) * 8.0)}

def reference(A_u, A_A_re, A_A_im, A_B_re, A_B_im, A_C_re, A_C_im, A_log_dt, A_Dskip, B_x, B_w_gate, B_w_up, B_w_down, c, w_mod):

    def _A_ref(u, A_re, A_im, B_re, B_im, C_re, C_im, log_dt, Dskip):
        Bsz, L, D = u.shape
        dt = torch.exp(log_dt)[:, None]
        A = A_re + 1j * A_im
        Bc = B_re + 1j * B_im
        Cc = C_re + 1j * C_im
        logdA = dt * A
        dB = dt * Bc
        l = (lambda x, dt: x.to(dt))(torch.arange(L), A_re.dtype)
        P = torch.exp(logdA[:, :, None] * l[None, None, :])
        K = torch.einsum('dn,dnl,dn->dl', Cc, P, dB).real
        L2 = 2 * L
        Kp = _pad(K, ((0, 0), (0, L)))
        up = (lambda x, *d: x.permute(*d))(u, 0, 2, 1)
        up = _pad(up, ((0, 0), (0, 0), (0, L)))
        Kf = (lambda x, axes=(-2, -1): torch.fft.rfft2(x.float(), dim=axes))(Kp[:, None, :], axes=(-2, -1))
        Uf = (lambda x, axes=(-2, -1): torch.fft.rfft2(x.float(), dim=axes))(up[:, :, None, :], axes=(-2, -1))
        Yf = Uf * Kf[None, :, :, :]
        y = (lambda x, s, axes=(-2, -1): torch.fft.irfft2(x, s=s, dim=axes).to(torch.bfloat16))(Yf, (1, L2), axes=(-2, -1))[:, :, 0, :L]
        y = (lambda x, *d: x.permute(*d))(y, 0, 2, 1) + u * Dskip[None, None, :]
        return y

    def _B_ref(x, w_gate, w_up, w_down):
        g = torch.einsum('bsd,df->bsf', x, w_gate)
        u = torch.einsum('bsd,df->bsf', x, w_up)
        h = _sigmoid(g) * u
        return torch.einsum('bsf,fd->bsd', h, w_down)
    x = A_u
    mod = (_silu(c) @ w_mod).reshape(c.shape[0], 6, 1, -1)
    sa, ca, ga, sb, cb, gb = (mod[:, 0].clone(), mod[:, 1].clone(), mod[:, 2].clone(), mod[:, 3].clone(), mod[:, 4].clone(), mod[:, 5].clone())
    h = _layernorm(x, axis=-1) * (1 + ca) + sa
    x = x + ga * _A_ref(h, A_A_re, A_A_im, A_B_re, A_B_im, A_C_re, A_C_im, A_log_dt, A_Dskip)
    h = _layernorm(x, axis=-1) * (1 + cb) + sb
    x = x + gb * _B_ref(h, B_w_gate, B_w_up, B_w_down)
    return x
```

```cpp
#include <hip/hip_runtime.h>
#include <hip/hip_bf16.h>
#include <stdint.h>

typedef unsigned short u16;
typedef unsigned int u32;
typedef float f32x2 __attribute__((ext_vector_type(2)));
typedef float f32x4 __attribute__((ext_vector_type(4)));
typedef float f32x16 __attribute__((ext_vector_type(16)));
typedef _Float16 f16x8 __attribute__((ext_vector_type(8)));
typedef _Float16 f16x2 __attribute__((ext_vector_type(2)));
typedef __bf16 bf16x2v __attribute__((ext_vector_type(2)));
typedef short s16x4 __attribute__((ext_vector_type(4)));
typedef short s16x8 __attribute__((ext_vector_type(8)));
typedef u32 u32x2 __attribute__((ext_vector_type(2)));
typedef u32 u32x4 __attribute__((ext_vector_type(4)));
#define LDSP __attribute__((address_space(3)))
#define DI __device__ __forceinline__

constexpr int L_ = 2048, D_ = 448, NS_ = 256, F_ = 1792;

constexpr size_t WS_MOD   = 0;
constexpr size_t WS_STAT  = 16384;
constexpr size_t WS_KSC   = WS_STAT + 16384;
constexpr size_t WS_KR    = WS_KSC + 4096;
constexpr int    KR_LEN   = 2064;
constexpr size_t WS_X1B   = WS_KR + (size_t)448 * KR_LEN * 2 + 1024;
constexpr size_t WS_HHF   = WS_X1B + (size_t)L_ * D_ * 2;
constexpr size_t WS_WDF   = WS_HHF + (size_t)L_ * F_ * 2;
constexpr size_t WS_END   = WS_WDF + (size_t)F_ * D_ * 2;

DI float bf2f(u16 v) { return __uint_as_float((u32)v << 16); }
DI float bflo(u32 v) { return __uint_as_float(v << 16); }
DI float bfhi(u32 v) { return __uint_as_float(v & 0xffff0000u); }
DI u32 pk_bf16(float a, float b) { f32x2 x = {a, b}; bf16x2v r = __builtin_convertvector(x, bf16x2v); return __builtin_bit_cast(u32, r); }
DI float rbf(float a) { return __uint_as_float(pk_bf16(0.f, a) & 0xffff0000u); }
DI u32 pk_f16(float a, float b) { f32x2 x = {a, b}; f16x2 r = __builtin_convertvector(x, f16x2); return __builtin_bit_cast(u32, r); }

DI float wave_sum(float v) {
#pragma unroll
  for (int o = 32; o > 0; o >>= 1) v += __shfl_xor(v, o);
  return v;
}

struct cf { float r, i; };
DI cf cmul(cf a, cf b) { cf c; c.r = a.r * b.r - a.i * b.i; c.i = a.r * b.i + a.i * b.r; return c; }
DI cf cexp_l(float zr, float zi, float l) {
  float e = __builtin_amdgcn_exp2f((zr * l) * 1.44269504088896341f);
  float rev = __builtin_amdgcn_fractf((zi * l) * 0.15915494309189535f);
  cf c; c.r = e * __builtin_amdgcn_cosf(rev); c.i = e * __builtin_amdgcn_sinf(rev); return c;
}
DI s16x4 tr_read(const void* p) { return __builtin_amdgcn_ds_read_tr16_b64_v4i16((LDSP s16x4*)p); }

constexpr int KG_ROW = 272;
__global__ __launch_bounds__(512, 2) void k_prep(
    const u16* __restrict__ x, const u16* __restrict__ Are, const u16* __restrict__ Aim, const u16* __restrict__ Bre,
    const u16* __restrict__ Bim, const u16* __restrict__ Cre, const u16* __restrict__ Cim, const u16* __restrict__ logdt,
    const u16* __restrict__ cvec, const u16* __restrict__ wmod, const u16* __restrict__ wdown, char* __restrict__ ws) {
  __shared__ __attribute__((aligned(16))) char smem[8 * 64 * KG_ROW + 4096];
  const int tid = threadIdx.x, lane = tid & 63, wv = tid >> 6, wg = blockIdx.x;
  float* modo = (float*)(ws + WS_MOD);
  float2* stat = (float2*)(ws + WS_STAT);
  char* tail = smem + 8 * 64 * KG_ROW;
  float* red = (float*)tail;

  const int ch = wv >> 2, d = 2 * wg + ch, n = tid & 255;
  const int pidx = d * NS_ + n;
  float dt = rbf(expf(bf2f(logdt[d])));
  float zr = dt * bf2f(Are[pidx]), zi = dt * bf2f(Aim[pidx]);
  float dBr = dt * bf2f(Bre[pidx]), dBi = dt * bf2f(Bim[pidx]);
  float Cr = bf2f(Cre[pidx]), Ci = bf2f(Cim[pidx]);
  cf W; W.r = Cr * dBr - Ci * dBi; W.i = Cr * dBi + Ci * dBr;

  for (int i = wv; i < 10; i += 8) {
    int r = wg + 224 * i;
    if (r < L_) {
      float s = 0.f, q = 0.f;
      if (lane < 56) {
        u32x4 v = *(const u32x4*)(x + (size_t)r * D_ + lane * 8);
#pragma unroll
        for (int j = 0; j < 4; ++j) { float a = bflo(v[j]), b = bfhi(v[j]); s += a + b; q += a * a + b * b; }
      }
      s = wave_sum(s); q = wave_sum(q);
      float mean = s * (1.f / D_);
      float var = fmaxf(q * (1.f / D_) - mean * mean, 0.f);
      float sd = rbf(sqrtf(rbf(rbf(var) + 1e-5f)));
      if (lane == 0) stat[r] = make_float2(rbf(mean), 1.0f / sd);
    }
  }
  {
    float acc[12];
#pragma unroll
    for (int j = 0; j < 12; ++j) acc[j] = 0.f;
    if (tid < D_) {
      float c = bf2f(cvec[tid]);
      float sl = rbf(c / (1.f + expf(-c)));
      const u32x2* p = (const u32x2*)(wmod + (size_t)tid * (6 * D_) + 12 * wg);
#pragma unroll
      for (int j = 0; j < 3; ++j) { u32x2 v = p[j];
        acc[4 * j + 0] = sl * bflo(v[0]); acc[4 * j + 1] = sl * bfhi(v[0]); acc[4 * j + 2] = sl * bflo(v[1]); acc[4 * j + 3] = sl * bfhi(v[1]); }
    }
#pragma unroll
    for (int j = 0; j < 12; ++j) acc[j] = wave_sum(acc[j]);
    if (lane == 0) {
#pragma unroll
      for (int j = 0; j < 12; ++j) red[64 + wv * 12 + j] = acc[j];
    }
  }
  if (wv < 7) {
    int fr = 7 * wg + wv, dtile = fr / 112, s = fr % 112;
    u32 o[4];
#pragma unroll
    for (int jj = 0; jj < 4; ++jj) {
      int j0 = 2 * jj, j1 = 2 * jj + 1;
      int f0 = 16 * s + 8 * (j0 >> 2) + 4 * (lane >> 5) + (j0 & 3), f1 = 16 * s + 8 * (j1 >> 2) + 4 * (lane >> 5) + (j1 & 3);
      int dd = 32 * dtile + (lane & 31);
      o[jj] = (u32)wdown[(size_t)f0 * D_ + dd] | ((u32)wdown[(size_t)f1 * D_ + dd] << 16);
    }
    *(u32x4*)(ws + WS_WDF + ((size_t)fr * 64 + lane) * 16) = u32x4{o[0], o[1], o[2], o[3]};
  }
  float sw = wave_sum(fabsf(W.r) + fabsf(W.i));
  if (lane == 0) red[wv] = sw;
  __syncthreads();
  if (tid < 12) { float s = 0.f; for (int w = 0; w < 8; ++w) s += red[64 + w * 12 + tid]; modo[12 * wg + tid] = rbf(s); }
  float S = red[4 * ch] + red[4 * ch + 1] + red[4 * ch + 2] + red[4 * ch + 3];
  int ex = (int)((__float_as_uint(S) >> 23) & 0xff) - 127 + 1;
  ex = S > 0.f ? ex : 0; ex = max(-100, min(100, ex));
  float sigma = __uint_as_float((u32)(127 + 14 - ex) << 23);
  W.r *= sigma; W.i *= sigma;

  char* row = smem + (wv * 64 + lane) * KG_ROW;
  {
    cf M[8]; M[1] = cexp_l(zr, zi, 1.f); M[2] = cmul(M[1], M[1]); M[3] = cmul(M[2], M[1]); M[4] = cmul(M[2], M[2]);
    M[5] = cmul(M[4], M[1]); M[6] = cmul(M[3], M[3]); M[7] = cmul(M[4], M[3]);
    cf M128 = cexp_l(zr, zi, 128.f), M8 = cexp_l(zr, zi, 8.f);
    cf R[48];
    cf G = W;
#pragma unroll
    for (int A = 0; A <= 16; ++A) {
      if (A < 2) { R[8 * A] = G;
#pragma unroll
        for (int c = 1; c < 8; ++c) R[8 * A + c] = cmul(G, M[c]); }
      else if (A < 4) { R[16 + 4 * (A - 2)] = G;
#pragma unroll
        for (int c = 1; c < 4; ++c) R[16 + 4 * (A - 2) + c] = cmul(G, M[2 * c]); }
      else if (A < 8) { R[24 + 2 * (A - 4)] = G; R[24 + 2 * (A - 4) + 1] = cmul(G, M[4]); }
      else R[32 + (A - 8)] = G;
      if (A < 16) G = cmul(G, M128);
    }
#pragma unroll
    for (int r = 41; r < 48; ++r) { R[r].r = 0.f; R[r].i = 0.f; }
#pragma unroll
    for (int c = 0; c < 6; ++c) {
      u32x4 vr, vi;
#pragma unroll
      for (int j = 0; j < 4; ++j) { vr[j] = pk_f16(R[8 * c + 2 * j].r, R[8 * c + 2 * j + 1].r); vi[j] = pk_f16(-R[8 * c + 2 * j].i, -R[8 * c + 2 * j + 1].i); }
      *(u32x4*)(row + 16 * c) = vr; *(u32x4*)(row + 96 + 16 * c) = vi;
    }
    cf Fv[16]; Fv[0].r = 1.f; Fv[0].i = 0.f; Fv[1] = M8;
#pragma unroll
    for (int b = 2; b < 16; ++b) Fv[b] = cmul(Fv[b - 1], M8);
#pragma unroll
    for (int c = 0; c < 2; ++c) {
      u32x4 vr, vi;
#pragma unroll
      for (int j = 0; j < 4; ++j) { vr[j] = pk_f16(Fv[8 * c + 2 * j].r, Fv[8 * c + 2 * j + 1].r); vi[j] = pk_f16(Fv[8 * c + 2 * j].i, Fv[8 * c + 2 * j + 1].i); }
      *(u32x4*)(row + 192 + 16 * c) = vr; *(u32x4*)(row + 224 + 16 * c) = vi;
    }
  }
  __builtin_amdgcn_wave_barrier();
  asm volatile("s_waitcnt lgkmcnt(0)" ::: "memory");
  f32x4 acc[3] = {};
  {
    const int g = lane >> 4, q = (lane & 15) >> 2, p = lane & 3;
    const char* wb = smem + (wv * 64) * KG_ROW;
#pragma unroll
    for (int pl = 0; pl < 2; ++pl)
#pragma unroll
      for (int s = 0; s < 2; ++s) {
        const char* r0 = wb + (32 * s + 8 * g + q) * KG_ROW + 8 * p;
        s16x4 b0 = tr_read(r0 + 192 + 32 * pl), b1 = tr_read(r0 + 4 * KG_ROW + 192 + 32 * pl);
        s16x8 bb = __builtin_shufflevector(b0, b1, 0, 1, 2, 3, 4, 5, 6, 7);
#pragma unroll
        for (int t = 0; t < 3; ++t) {
          s16x4 a0 = tr_read(r0 + 96 * pl + 32 * t), a1 = tr_read(r0 + 4 * KG_ROW + 96 * pl + 32 * t);
          s16x8 aa = __builtin_shufflevector(a0, a1, 0, 1, 2, 3, 4, 5, 6, 7);
          acc[t] = __builtin_amdgcn_mfma_f32_16x16x32_f16(__builtin_bit_cast(f16x8, aa), __builtin_bit_cast(f16x8, bb), acc[t], 0, 0, 0);
        }
      }
  }
  __syncthreads();
  float* red2 = (float*)smem;
#pragma unroll
  for (int t = 0; t < 3; ++t) *(f32x4*)(red2 + ((wv * 3 + t) * 64 + lane) * 4) = acc[t];
  __syncthreads();
  _Float16* Kd = (_Float16*)(smem + 32768) + ch * 768;
  {
    int i = tid & 255;
#pragma unroll
    for (int t = 0; t < 3; ++t) {
      int ln = i & 63, rg = i >> 6; float s = 0.f;
#pragma unroll
      for (int w = 0; w < 4; ++w) s += red2[(((4 * ch + w) * 3 + t) * 64 + ln) * 4 + rg];
      int rho = 16 * t + 4 * (ln >> 4) + rg, b = ln & 15;
      Kd[rho * 16 + b] = (_Float16)s;
    }
  }
  __syncthreads();
  {
    int q = tid & 255;
    u16 o[8];
#pragma unroll
    for (int j = 0; j < 8; ++j) {
      int l = 2047 - 8 * q - j;
      u32 u = __float_as_uint((float)l); u = (u + 0x7fffu + ((u >> 16) & 1u)) & 0xffff0000u;
      int lp = (int)__uint_as_float(u);
      int b = (lp >> 3) & 15, rho;
      if (lp < 256) rho = ((lp >> 7) << 3) + (lp & 7);
      else if (lp < 512) rho = 16 + (((lp >> 7) & 1) << 2) + ((lp >> 1) & 3);
      else if (lp < 1024) rho = 24 + (((lp >> 7) & 3) << 1) + ((lp >> 2) & 1);
      else rho = 32 + ((lp - 1024) >> 7);
      o[j] = __builtin_bit_cast(u16, Kd[rho * 16 + b]);
    }
    u16* kr = (u16*)(ws + WS_KR) + (size_t)d * KR_LEN;
    *(u32x4*)(kr + 8 * q) = u32x4{(u32)o[0] | ((u32)o[1] << 16), (u32)o[2] | ((u32)o[3] << 16), (u32)o[4] | ((u32)o[5] << 16), (u32)o[6] | ((u32)o[7] << 16)};
    if (q < 2) *(u32x4*)(kr + 2048 + 8 * q) = u32x4{0, 0, 0, 0};
    if (q == 0) ((float*)(ws + WS_KSC))[d] = 1.0f / sigma;
  }
}

constexpr int CS = 4160;
constexpr int CV_KC = 0;
constexpr int CV_H = 2 * 8 * CS;
constexpr int CV_RED = 0;
constexpr int CV_LDS = 8 * 128 * 80 > CV_H + 2 * 4608 ? 8 * 128 * 80 : CV_H + 2 * 4608;
__global__ __launch_bounds__(512, 2) void k_conv(const u16* __restrict__ x, const u16* __restrict__ dskip, char* __restrict__ ws) {
  __shared__ __attribute__((aligned(16))) char smem[CV_LDS];
  const int tid = threadIdx.x, lane = tid & 63, wv = tid >> 6, wg = blockIdx.x;
  const int d0 = 2 * wg;
  const float* mod = (const float*)(ws + WS_MOD);
  const float2* stat = (const float2*)(ws + WS_STAT);
  u32 xv[4]; float2 st[4];
#pragma unroll
  for (int i = 0; i < 4; ++i) { int t = tid + 512 * i; xv[i] = *(const u32*)(x + (size_t)t * D_ + d0); st[i] = stat[t]; }
  const int ch = wv >> 2, q = tid & 255;
  const u16* kr = (const u16*)(ws + WS_KR) + (size_t)(d0 + ch) * KR_LEN + 8 * q;
  u32x4 k0v = *(const u32x4*)kr, k1v = *(const u32x4*)(kr + 8);
  float sa[2], c1[2], ga[2], dsk[2], tau[2], post[2];
#pragma unroll
  for (int c = 0; c < 2; ++c) {
    sa[c] = mod[d0 + c]; c1[c] = rbf(1.f + mod[D_ + d0 + c]); ga[c] = mod[2 * D_ + d0 + c]; dsk[c] = bf2f(dskip[d0 + c]);
    float bound = 21.25f * fabsf(c1[c]) * 1.02f + fabsf(sa[c]) * 1.02f;
    int ex = (int)((__float_as_uint(bound) >> 23) & 0xff) - 127 + 1; ex = bound > 0.f ? ex : 0; ex = max(-100, min(100, ex));
    tau[c] = __uint_as_float((u32)(127 + 15 - ex) << 23);
    post[c] = ((const float*)(ws + WS_KSC))[d0 + c] * __uint_as_float((u32)(127 - 15 + ex) << 23);
  }
  {
    u32 w[8] = {k0v[0], k0v[1], k0v[2], k0v[3], k1v[0], k1v[1], k1v[2], k1v[3]};
    u32 o[7];
#pragma unroll
    for (int i = 0; i < 7; ++i) o[i] = __builtin_amdgcn_alignbit(w[i + 1], w[i], 16);
    char* base = smem + CV_KC + ch * 8 * CS + q * 16;
#pragma unroll
    for (int s = 0; s < 8; ++s) {
      u32x4 v;
      if (s & 1) v = u32x4{o[s >> 1], o[(s >> 1) + 1], o[(s >> 1) + 2], o[(s >> 1) + 3]};
      else v = u32x4{w[s >> 1], w[(s >> 1) + 1], w[(s >> 1) + 2], w[(s >> 1) + 3]};
      *(u32x4*)(base + s * CS) = v;
    }
    if (q < 8) *(u32x4*)(smem + CV_KC + ch * 8 * CS + q * CS + 256 * 16) = u32x4{0, 0, 0, 0};
    if (q < 32) *(u32x4*)(smem + CV_H + ch * 4608 + q * 16) = u32x4{0, 0, 0, 0};
  }
  float hv[4][2];
#pragma unroll
  for (int i = 0; i < 4; ++i) {
    int t = tid + 512 * i;
#pragma unroll
    for (int c = 0; c < 2; ++c) {
      float xx = c ? bfhi(xv[i]) : bflo(xv[i]);
      float a = rbf(xx - st[i].x);
      float nn = rbf(a * st[i].y);
      float h = rbf(rbf(nn * c1[c]) + sa[c]);
      hv[i][c] = h;
      *(_Float16*)(smem + CV_H + c * 4608 + (256 + t) * 2) = (_Float16)(h * tau[c]);
    }
  }
  __syncthreads();
  f32x4 acc[8] = {};
  {
    const int g = lane >> 4, b = lane & 15;
#pragma unroll
    for (int kk = 0; kk < 2; ++kk) {
      const int k0 = 4 * (wv & 3) + 2 * kk;
      const char* ab = smem + CV_KC + ch * 8 * CS + (7 - (b & 7)) * CS + 2 * (2040 - 16 * k0 - 16 * (g >> 1) + 8 * (g & 1) - 8 * (b >> 3) - 256 * 7);
      const char* fb = smem + CV_H + ch * 4608 + 2 * (256 + 16 * (b - k0 - (g >> 1)) + 8 * (g & 1));
      f16x8 A[8], Fm[8];
#pragma unroll
      for (int i = 0; i < 8; ++i) { A[i] = *(const f16x8*)(ab + 512 * (7 - i)); Fm[i] = *(const f16x8*)(fb + 512 * i); }
#pragma unroll
      for (int c = 0; c < 8; ++c)
#pragma unroll
        for (int i = 0; i <= c; ++i) acc[c] = __builtin_amdgcn_mfma_f32_16x16x32_f16(A[i], Fm[c - i], acc[c], 0, 0, 0);
    }
  }
  __syncthreads();
  {
    float* red = (float*)(smem + CV_RED) + wv * (128 * 20);
    const int g = lane >> 4, a = lane & 15;
#pragma unroll
    for (int c = 0; c < 8; ++c) *(f32x4*)(red + (16 * c + a) * 20 + 4 * g) = acc[c];
  }
  __syncthreads();
  u16* x1b = (u16*)(ws + WS_X1B);
#pragma unroll
  for (int i = 0; i < 4; ++i) {
    int t = tid + 512 * i;
#pragma unroll
    for (int c = 0; c < 2; ++c) {
      const float* red = (const float*)(smem + CV_RED) + (4 * c) * (128 * 20) + (t >> 4) * 20 + (t & 15);
      float y = red[0] + red[128 * 20] + red[2 * 128 * 20] + red[3 * 128 * 20];
      y = rbf(y * post[c]);
      float y2 = rbf(y + rbf(hv[i][c] * dsk[c]));
      float xx = c ? bfhi(xv[i]) : bflo(xv[i]);
      float x1 = xx + rbf(ga[c] * y2);
      x1b[((size_t)(t >> 6) * D_ + d0 + c) * 64 + (t & 63)] = (u16)(pk_bf16(x1, 0.f) & 0xffffu);
    }
  }
}

constexpr int G1_H = 0;
constexpr int G1_W = 448 * 128;
constexpr int G1_WCH = 32 * 896;
constexpr int G1_LDS = G1_W + 3 * G1_WCH;
__global__ __launch_bounds__(512, 2) void k_gemm1(const u16* __restrict__ wgate, const u16* __restrict__ wup, char* __restrict__ ws) {
  __shared__ __attribute__((aligned(16))) char smem[G1_LDS];
  const int tid = threadIdx.x, lane = tid & 63, wv = tid >> 6;
  const int mt = blockIdx.x >> 3, nt = blockIdx.x & 7;
  const float* mod = (const float*)(ws + WS_MOD);
  const u16* wsrc[4];
#pragma unroll
  for (int i = 0; i < 4; ++i) {
    int u = (wv + 8 * i) * 64 + lane; u = u < 1792 ? u : 0;
    int row = u / 56, pos = u % 56, tp = pos >> 1, half = pos & 1;
    int f = ((row >> 1) & 1) + 2 * ((row >> 3) & 1);
    int tau = tp - f; tau = tau < 0 ? tau + 28 : tau;
    wsrc[i] = (half ? wup : wgate) + (size_t)row * F_ + 224 * nt + 8 * tau;
  }
  auto stage = [&](int ks, int slot) {
#pragma unroll
    for (int i = 0; i < 4; ++i) {
      if (wv + 8 * i < 28)
        __builtin_amdgcn_global_load_lds((const u32*)(wsrc[i] + (size_t)ks * 32 * F_), (LDSP u32*)(smem + G1_W + slot * G1_WCH + (wv + 8 * i) * 1024), 16, 0, 0);
    }
  };
  const int dl = tid >> 4, tq = tid & 15;
  const u16* xs = (const u16*)(ws + WS_X1B) + ((size_t)mt * D_ + dl) * 64 + 4 * tq;
  u32x2 xr[14];
#pragma unroll
  for (int i = 0; i < 14; ++i) xr[i] = *(const u32x2*)(xs + (size_t)i * 32 * 64);
  float c2[14], sb[14];
#pragma unroll
  for (int i = 0; i < 14; ++i) { c2[i] = rbf(1.f + mod[4 * D_ + 32 * i + dl]); sb[i] = mod[3 * D_ + 32 * i + dl]; }
  stage(0, 0); stage(1, 1);
  float s[4] = {0, 0, 0, 0}, q[4] = {0, 0, 0, 0};
#pragma unroll
  for (int i = 0; i < 14; ++i) {
    float v[4] = {bflo(xr[i][0]), bfhi(xr[i][0]), bflo(xr[i][1]), bfhi(xr[i][1])};
#pragma unroll
    for (int j = 0; j < 4; ++j) { s[j] += v[j]; q[j] += v[j] * v[j]; }
  }
#pragma unroll
  for (int j = 0; j < 4; ++j) { s[j] += __shfl_xor(s[j], 16); s[j] += __shfl_xor(s[j], 32); q[j] += __shfl_xor(q[j], 16); q[j] += __shfl_xor(q[j], 32); }
  float* red = (float*)(smem + G1_W + 2 * G1_WCH);
  if (lane < 16) {
#pragma unroll
    for (int j = 0; j < 4; ++j) { red[(wv * 64 + 4 * tq + j) * 2] = s[j]; red[(wv * 64 + 4 * tq + j) * 2 + 1] = q[j]; }
  }
  asm volatile("s_waitcnt lgkmcnt(0)" ::: "memory"); __builtin_amdgcn_s_barrier();
  float mean[4], rs[4];
#pragma unroll
  for (int j = 0; j < 4; ++j) {
    float ss = 0.f, qq = 0.f;
#pragma unroll
    for (int w = 0; w < 8; ++w) { ss += red[(w * 64 + 4 * tq + j) * 2]; qq += red[(w * 64 + 4 * tq + j) * 2 + 1]; }
    float m = ss * (1.f / D_); float var = fmaxf(qq * (1.f / D_) - m * m, 0.f);
    mean[j] = rbf(m); rs[j] = 1.0f / rbf(sqrtf(rbf(rbf(var) + 1e-5f)));
  }
#pragma unroll
  for (int i = 0; i < 14; ++i) {
    float v[4] = {bflo(xr[i][0]), bfhi(xr[i][0]), bflo(xr[i][1]), bfhi(xr[i][1])}, h[4];
#pragma unroll
    for (int j = 0; j < 4; ++j) h[j] = rbf(rbf(rbf(v[j] - mean[j]) * rs[j]) * c2[i]) + sb[i];
    int dd = 32 * i + dl;
    int fz = ((dd >> 1) & 1) | (((dd >> 3) & 1) << 1);
    *(u32x2*)(smem + G1_H + dd * 128 + (((tq >> 2) ^ fz) << 5) + ((tq & 3) << 3)) = u32x2{pk_bf16(h[0], h[1]), pk_bf16(h[2], h[3])};
  }
  const int th = wv & 1, cg = wv >> 1, wvu = __builtin_amdgcn_readfirstlane(wv);
  const int g = lane >> 4, qq4 = (lane & 15) >> 2, p = lane & 3;
  f32x4 acc[7][2] = {};
  for (int ks = 0; ks < 14; ++ks) {
    if (ks == 13) asm volatile("s_waitcnt vmcnt(0) lgkmcnt(0)" ::: "memory");
    else if (wvu < 4) asm volatile("s_waitcnt vmcnt(4) lgkmcnt(0)" ::: "memory");
    else asm volatile("s_waitcnt vmcnt(3) lgkmcnt(0)" ::: "memory");
    __builtin_amdgcn_s_barrier();
    if (ks + 2 < 14) stage(ks + 2, (ks + 2) % 3);
    const char* wb = smem + G1_W + (ks % 3) * G1_WCH;
    s16x8 bfr[2];
#pragma unroll
    for (int tt = 0; tt < 2; ++tt) {
      s16x4 lo, hi;
      { int r = 8 * g + qq4; int fz = ((r >> 1) & 1) | (((r >> 3) & 1) << 1);
        lo = tr_read(smem + G1_H + (32 * ks + r) * 128 + (((2 * th + tt) ^ fz) << 5) + 8 * p); }
      { int r = 8 * g + 4 + qq4; int fz = ((r >> 1) & 1) | (((r >> 3) & 1) << 1);
        hi = tr_read(smem + G1_H + (32 * ks + r) * 128 + (((2 * th + tt) ^ fz) << 5) + 8 * p); }
      bfr[tt] = __builtin_shufflevector(lo, hi, 0, 1, 2, 3, 4, 5, 6, 7);
    }
#pragma unroll
    for (int t = 0; t < 7; ++t) {
      int tau = 7 * cg + t;
      s16x4 lo, hi;
      { int r = 8 * g + qq4; int tp = tau + ((r >> 1) & 1) + 2 * ((r >> 3) & 1); tp = tp >= 28 ? tp - 28 : tp;
        lo = tr_read(wb + r * 896 + tp * 32 + (p & 1) * 16 + (p >> 1) * 8); }
      { int r = 8 * g + 4 + qq4; int tp = tau + ((r >> 1) & 1) + 2 * ((r >> 3) & 1); tp = tp >= 28 ? tp - 28 : tp;
        hi = tr_read(wb + r * 896 + tp * 32 + (p & 1) * 16 + (p >> 1) * 8); }
      s16x8 af = __builtin_shufflevector(lo, hi, 0, 1, 2, 3, 4, 5, 6, 7);
#pragma unroll
      for (int tt = 0; tt < 2; ++tt)
        acc[t][tt] = __builtin_amdgcn_mfma_f32_16x16x32_bf16(af, bfr[tt], acc[t][tt], 0, 0, 0);
    }
  }
  char* hhf = ws + WS_HHF;
  const int tile32 = 2 * mt + th;
#pragma unroll
  for (int t = 0; t < 7; ++t) {
    float hh[4];
#pragma unroll
    for (int r = 0; r < 4; ++r) {
      auto sw = __builtin_amdgcn_permlane16_swap(__float_as_uint(acc[t][0][r]), __float_as_uint(acc[t][1][r]), false, false);
      float gg = rbf(__uint_as_float(sw[0])), uu = rbf(__uint_as_float(sw[1]));
      float sg = rbf(__builtin_amdgcn_rcpf(1.f + __builtin_amdgcn_exp2f(-1.44269504088896341f * gg)));
      hh[r] = sg * uu;
    }
    int tg = 28 * nt + 7 * cg + t;
    *(u32x2*)(hhf + (((size_t)tile32 * 112 + (tg >> 1)) * 64 + lane) * 16 + (tg & 1) * 8) = u32x2{pk_bf16(hh[0], hh[1]), pk_bf16(hh[2], hh[3])};
  }
}

__global__ __launch_bounds__(256, 1) void k_gemm2(u16* __restrict__ out, const char* __restrict__ ws) {
  __shared__ __attribute__((aligned(16))) char smem[4 * 64 * 68 * 4];
  const int tid = threadIdx.x, lane = tid & 63, wv = tid >> 6;
  const int mt = blockIdx.x / 7, nt = blockIdx.x % 7;
  const float* mod = (const float*)(ws + WS_MOD);
  const char* ha = ws + WS_HHF + (((size_t)(2 * mt) * 112 + 28 * wv) * 64 + lane) * 16;
  const char* wb = ws + WS_WDF + (((size_t)(2 * nt) * 112 + 28 * wv) * 64 + lane) * 16;
  f32x16 acc[2][2] = {};
#pragma unroll
  for (int s = 0; s < 28; ++s) {
    s16x8 a0 = *(const s16x8*)(ha + s * 1024), a1 = *(const s16x8*)(ha + 112 * 1024 + s * 1024);
    s16x8 b0 = *(const s16x8*)(wb + s * 1024), b1 = *(const s16x8*)(wb + 112 * 1024 + s * 1024);
    acc[0][0] = __builtin_amdgcn_mfma_f32_32x32x16_bf16(a0, b0, acc[0][0], 0, 0, 0);
    acc[0][1] = __builtin_amdgcn_mfma_f32_32x32x16_bf16(a0, b1, acc[0][1], 0, 0, 0);
    acc[1][0] = __builtin_amdgcn_mfma_f32_32x32x16_bf16(a1, b0, acc[1][0], 0, 0, 0);
    acc[1][1] = __builtin_amdgcn_mfma_f32_32x32x16_bf16(a1, b1, acc[1][1], 0, 0, 0);
  }
  float* red = (float*)smem + wv * (64 * 68);
#pragma unroll
  for (int a = 0; a < 2; ++a)
#pragma unroll
    for (int b = 0; b < 2; ++b)
#pragma unroll
      for (int r4 = 0; r4 < 4; ++r4) {
        f32x4 v = {acc[a][b][4 * r4], acc[a][b][4 * r4 + 1], acc[a][b][4 * r4 + 2], acc[a][b][4 * r4 + 3]};
        *(f32x4*)(red + (32 * b + (lane & 31)) * 68 + 32 * a + 8 * r4 + 4 * (lane >> 5)) = v;
      }
  __syncthreads();
  {
    const int t = tid >> 2, dc = (tid & 3) * 16;
    const float* r0 = (const float*)smem;
    const u16* x1b = (const u16*)(ws + WS_X1B) + ((size_t)mt * D_ + 64 * nt) * 64;
    u32 o[8];
#pragma unroll
    for (int j = 0; j < 16; j += 2) {
      float res[2];
#pragma unroll
      for (int e = 0; e < 2; ++e) {
        int dd = dc + j + e;
        float v = r0[dd * 68 + t] + r0[64 * 68 + dd * 68 + t] + r0[2 * 64 * 68 + dd * 68 + t] + r0[3 * 64 * 68 + dd * 68 + t];
        float gb = mod[5 * D_ + 64 * nt + dd];
        res[e] = bf2f(x1b[dd * 64 + t]) + rbf(gb * rbf(v));
      }
      o[j >> 1] = pk_bf16(res[0], res[1]);
    }
    u16* op = out + (size_t)(64 * mt + t) * D_ + 64 * nt + dc;
    *(u32x4*)op = u32x4{o[0], o[1], o[2], o[3]};
    *(u32x4*)(op + 8) = u32x4{o[4], o[5], o[6], o[7]};
  }
}

extern "C" void kernel_launch(void* const* d_in, const int* in_sizes, int n_in, void* d_out, int out_size, void* d_ws, size_t ws_size,
                              hipStream_t stream) {
  const u16* x = (const u16*)d_in[0];
  char* ws = (char*)d_ws;
  k_prep<<<224, 512, 0, stream>>>(x, (const u16*)d_in[1], (const u16*)d_in[2], (const u16*)d_in[3], (const u16*)d_in[4], (const u16*)d_in[5],
                                  (const u16*)d_in[6], (const u16*)d_in[7], (const u16*)d_in[13], (const u16*)d_in[14], (const u16*)d_in[12], ws);
  k_conv<<<224, 512, 0, stream>>>(x, (const u16*)d_in[8], ws);
  k_gemm1<<<256, 512, 0, stream>>>((const u16*)d_in[10], (const u16*)d_in[11], ws);
  k_gemm2<<<224, 256, 0, stream>>>((u16*)d_out, ws);
}
```

```cpp
#define OUT_AUX 18
#define WDT_POS 9
#define NWDA 4
#define WDA_POS 1
#define HFW 0
#define W1P1 1
#define HP1 1
#define PRIO_P 2
#define PRIO_T 1
#define LVL 15
#define OW 2
#define OW_AUX 18
#define G2P 6
#define ROT 0
#define ST_AUX 16
#define HH_AUX 16
#include <hip/hip_runtime.h>
#include <hip/hip_bf16.h>
#include <stdint.h>
#include <utility>

typedef unsigned short u16;
typedef unsigned int u32;
typedef unsigned long long u64;
typedef float f32x2 __attribute__((ext_vector_type(2)));
typedef float f32x4 __attribute__((ext_vector_type(4)));
typedef float f32x16 __attribute__((ext_vector_type(16)));
typedef _Float16 f16x8 __attribute__((ext_vector_type(8)));
typedef _Float16 f16x2 __attribute__((ext_vector_type(2)));
typedef __bf16 bf16x2v __attribute__((ext_vector_type(2)));
typedef short s16x4 __attribute__((ext_vector_type(4)));
typedef short s16x8 __attribute__((ext_vector_type(8)));
typedef u32 u32x2 __attribute__((ext_vector_type(2)));
typedef u32 u32x4 __attribute__((ext_vector_type(4)));
#define LDSP __attribute__((address_space(3)))
#define GAS __attribute__((address_space(1)))
typedef GAS u32 gu32;
typedef GAS u64 gu64;
#define RLX_AGENT __ATOMIC_RELAXED, __HIP_MEMORY_SCOPE_AGENT
#define DI __device__ __forceinline__

constexpr int L_ = 2048, D_ = 448, NS_ = 256, F_ = 1792;

constexpr size_t WS_MOD   = 0;
constexpr size_t WS_EPOCH = 12288;
constexpr size_t WS_STG   = 16384;
constexpr size_t WS_MODG  = WS_STG + 16384;
constexpr size_t WS_KSC   = WS_MODG + 24576;
constexpr size_t WS_KR    = WS_KSC + 4096;
constexpr int    KR_LEN   = 2064;
constexpr size_t WS_X1G   = WS_KR + (size_t)448 * KR_LEN * 2 + 1024;
constexpr size_t WS_HHF   = WS_X1G + (size_t)L_ * D_ * 2;
constexpr size_t WS_WDF   = WS_HHF + (size_t)L_ * F_ * 2;
constexpr size_t WS_W1P   = WS_WDF + (size_t)F_ * D_ * 2;
constexpr size_t WS_C2SB  = WS_W1P + (size_t)2 * F_ * D_ * 2;
constexpr size_t WS_XG    = WS_C2SB + 4096;
constexpr size_t WS_HFLG  = WS_XG + (size_t)224 * 16384;
constexpr size_t WS_PKF   = WS_HFLG + 32768;
constexpr size_t WS_ARR   = WS_PKF + 32768;
constexpr size_t WS_DBG   = WS_ARR + 4096;
constexpr size_t WS_END   = WS_DBG + 4 * 256 * 8 * 16 * 8;

#define STAMP_DECL
#define STAMP(i)
#define STAMP_RT(i)
#define STAMP_OUT(kid, ws)

DI float bflo(u32 v) { return __uint_as_float(v << 16); }
DI float bfhi(u32 v) { return __uint_as_float(v & 0xffff0000u); }
DI float bf2f(u16 v) { return __uint_as_float((u32)v << 16); }
DI u32 pk_bf16(float a, float b) { f32x2 x = {a, b}; bf16x2v r = __builtin_convertvector(x, bf16x2v); return __builtin_bit_cast(u32, r); }
DI float rbf(float a) { return __uint_as_float(pk_bf16(0.f, a)); }
DI u32 pk_f16(float a, float b) { f32x2 x = {a, b}; f16x2 r = __builtin_convertvector(x, f16x2); return __builtin_bit_cast(u32, r); }
DI u32 umn(u32 a, u32 b) { return a < b ? a : b; }
DI u32 umx(u32 a, u32 b) { return a > b ? a : b; }
DI u32 umx4(u32x4 v) { return umx(umx(v[0], v[1]), umx(v[2], v[3])); }
DI u32 f2u(float a) { return __float_as_uint(a); }
DI float u2f(u32 a) { return __uint_as_float(a); }
#define SB() __builtin_amdgcn_sched_barrier(0)
#define KEEP(x) asm volatile("" :: "v"(x))
template <int... I, class F> DI void static_for_impl(std::integer_sequence<int, I...>, F&& f) { (f(std::integral_constant<int, I>{}), ...); }
template <int N, class F> DI void static_for(F&& f) { static_for_impl(std::make_integer_sequence<int, N>{}, f); }
typedef __amdgpu_buffer_rsrc_t rsrc_t;
DI u32x4 bload(rsrc_t r, u32 voff, u32 soff) { return __builtin_bit_cast(u32x4, __builtin_amdgcn_raw_buffer_load_b128(r, voff, soff, 0)); }
DI u32x4 bload_sc1(rsrc_t r, u32 voff, u32 soff) { return __builtin_bit_cast(u32x4, __builtin_amdgcn_raw_buffer_load_b128(r, voff, soff, 16)); }
DI float dot2bf(u32 a, u32 b, float c) { return __builtin_amdgcn_fdot2_f32_bf16(__builtin_bit_cast(bf16x2v, a), __builtin_bit_cast(bf16x2v, b), c, false); }
template <int CTRL> DI float dpp_add(float v) { return v + __uint_as_float(__builtin_amdgcn_update_dpp(0u, __float_as_uint(v), CTRL, 0xf, 0xf, false)); }
DI s16x4 tr_read(const void* p) { return __builtin_amdgcn_ds_read_tr16_b64_v4i16((LDSP s16x4*)p); }
DI s16x8 cat8(s16x4 lo, s16x4 hi) { return __builtin_shufflevector(lo, hi, 0, 1, 2, 3, 4, 5, 6, 7); }
DI float wave_sum(float v) {
  auto t = __builtin_amdgcn_permlane32_swap(f2u(v), f2u(v), false, false); v = u2f(t[0]) + u2f(t[1]);
  auto s = __builtin_amdgcn_permlane16_swap(f2u(v), f2u(v), false, false); v = u2f(s[0]) + u2f(s[1]);
  v = dpp_add<0x128>(v); v = dpp_add<0x124>(v); v = dpp_add<0x122>(v); v = dpp_add<0x121>(v);
  return v;
}
typedef f32x2 cf;
DI cf cmul(cf a, cf b) {
  cf d;
  asm("v_pk_mul_f32 %0, %1, %2 op_sel:[0,0] op_sel_hi:[0,1]\n\t"
      "v_pk_fma_f32 %0, %1, %2, %0 op_sel:[1,1,0] op_sel_hi:[1,0,1] neg_lo:[0,1,0]" : "=&v"(d) : "v"(a), "v"(b));
  return d;
}
DI void cmul3(cf g, cf m0, cf m1, cf m2, cf& d0, cf& d1, cf& d2) {
  asm("v_pk_mul_f32 %0, %3, %4 op_sel:[0,0] op_sel_hi:[0,1]\n\t"
      "v_pk_mul_f32 %1, %3, %5 op_sel:[0,0] op_sel_hi:[0,1]\n\t"
      "v_pk_mul_f32 %2, %3, %6 op_sel:[0,0] op_sel_hi:[0,1]\n\t"
      "v_pk_fma_f32 %0, %3, %4, %0 op_sel:[1,1,0] op_sel_hi:[1,0,1] neg_lo:[0,1,0]\n\t"
      "v_pk_fma_f32 %1, %3, %5, %1 op_sel:[1,1,0] op_sel_hi:[1,0,1] neg_lo:[0,1,0]\n\t"
      "v_pk_fma_f32 %2, %3, %6, %2 op_sel:[1,1,0] op_sel_hi:[1,0,1] neg_lo:[0,1,0]"
      : "=&v"(d0), "=&v"(d1), "=&v"(d2) : "v"(g), "v"(m0), "v"(m1), "v"(m2));
}
DI void cmul4(cf g, cf m0, cf m1, cf m2, cf m3, cf& d0, cf& d1, cf& d2, cf& d3) {
  asm("v_pk_mul_f32 %0, %4, %5 op_sel:[0,0] op_sel_hi:[0,1]\n\t"
      "v_pk_mul_f32 %1, %4, %6 op_sel:[0,0] op_sel_hi:[0,1]\n\t"
      "v_pk_mul_f32 %2, %4, %7 op_sel:[0,0] op_sel_hi:[0,1]\n\t"
      "v_pk_mul_f32 %3, %4, %8 op_sel:[0,0] op_sel_hi:[0,1]\n\t"
      "v_pk_fma_f32 %0, %4, %5, %0 op_sel:[1,1,0] op_sel_hi:[1,0,1] neg_lo:[0,1,0]\n\t"
      "v_pk_fma_f32 %1, %4, %6, %1 op_sel:[1,1,0] op_sel_hi:[1,0,1] neg_lo:[0,1,0]\n\t"
      "v_pk_fma_f32 %2, %4, %7, %2 op_sel:[1,1,0] op_sel_hi:[1,0,1] neg_lo:[0,1,0]\n\t"
      "v_pk_fma_f32 %3, %4, %8, %3 op_sel:[1,1,0] op_sel_hi:[1,0,1] neg_lo:[0,1,0]"
      : "=&v"(d0), "=&v"(d1), "=&v"(d2), "=&v"(d3) : "v"(g), "v"(m0), "v"(m1), "v"(m2), "v"(m3));
}
DI cf cexp_l(float zr, float zi, float l, float sgn) {
  float e = __builtin_amdgcn_exp2f((zr * l) * 1.44269504088896341f);
  float rev = __builtin_amdgcn_fractf((zi * l) * 0.15915494309189535f);
  return cf{e * __builtin_amdgcn_cosf(rev), (sgn * e) * __builtin_amdgcn_sinf(rev)};
}
typedef unsigned short u16x2 __attribute__((ext_vector_type(2)));
DI int exp_bound(float s) {
  int ex = (int)((f2u(s) >> 23) & 0xff) - 126; ex = s > 0.f ? ex : 0; return max(-100, min(100, ex));
}
DI float pow2i(int e) { return u2f((u32)(127 + e) << 23); }

template <int AUX> DI void bstore_a(u32x4 v, rsrc_t r, u32 off) { __builtin_amdgcn_raw_buffer_store_b128(__builtin_bit_cast(__attribute__((ext_vector_type(4))) int, v), r, off, 0, AUX); }
DI void bstore(u32x4 v, rsrc_t r, u32 voff, u32 soff) { bstore_a<ST_AUX>(v, r, voff + soff); }

constexpr int PK_UNITS1 = 28 * 28, PK_UNITS = PK_UNITS1 + 7 * 56;
DI int pk_owner(int u) { return u < 896 ? u >> 2 : 224 + (((u - 896) % 168) >> 3); }
constexpr u32 TAG_C = 0x9E3779B9u;
constexpr u32 X1_VOID = 0xffffffffu;
constexpr u32 SPIN_CAP = 1u << 16;
constexpr u32 OOB = 0x80000000u;
DI void pack_load2(const u16* __restrict__ wgate, const u16* __restrict__ wup, const u16* __restrict__ wdown, int u, u32 mask, int lane, u32x4 (&v)[4]) {
  const bool w1 = u < PK_UNITS1;
  const int u2 = u - PK_UNITS1;
  const rsrc_t ra = __builtin_amdgcn_make_buffer_rsrc((void*)(w1 ? wgate : wdown), 0, D_ * F_ * 2, 0x00020000);
  const rsrc_t rb = __builtin_amdgcn_make_buffer_rsrc((void*)(w1 ? wup : wdown), 0, D_ * F_ * 2, 0x00020000);
  const u32 rowb = w1 ? F_ * 2 : D_ * 2;
  const u32 vo = ((lane >> 3) * rowb + (lane & 7) * 16) | mask;
  const u32 so = w1 ? (16 * (u % 28) * F_ + 64 * (u / 28)) * 2 : (32 * (u2 % 56) * D_ + 64 * (u2 / 56)) * 2;
  const u32 sb = w1 ? so : so + 16 * rowb;
  v[0] = bload(ra, vo, so); v[1] = bload(ra, vo, so + 8 * rowb);
  v[2] = bload(rb, vo, sb); v[3] = bload(rb, vo, sb + 8 * rowb);
}
DI void pack_wr(char* lds_wave, int lane, const u32x4 (&v)[4]) {
#pragma unroll
  for (int q = 0; q < 4; ++q) *(u32x4*)(lds_wave + q * 1024 + lane * 16) = v[q];
  __builtin_amdgcn_wave_barrier();
}
DI void pack_rd(const char* lds_wave, int u, int lane, u32x4 (&f)[4]) {
  const int g16 = lane >> 4, r = (lane >> 2) & 3, c = lane & 3;
  const bool w1 = u < PK_UNITS1;
  const char* base = lds_wave + (w1 ? (c >> 1) * 2048 + (8 * (g16 >> 1) + r) * 128 + (8 * (c & 1) + 4 * (g16 & 1)) * 2
                                    : (8 * (g16 >> 1) + r) * 128 + (16 * (g16 & 1) + 4 * c) * 2);
  const int s1 = w1 ? 32 : 64, s2 = w1 ? 64 : 2048;
#pragma unroll
  for (int q = 0; q < 4; ++q) {
    const char* p = base + (q & 1) * s1 + (q >> 1) * s2;
    f[q] = __builtin_bit_cast(u32x4, cat8(tr_read(p), tr_read(p + 512)));
  }
  __builtin_amdgcn_wave_barrier();
}
DI void pack_st(rsrc_t rs_ws, int u, u32 mask, int lane, const u32x4 (&f)[4]) {
  const u32 vo = (lane * 16) | mask;
  const bool w1 = u < PK_UNITS1;
  const int u2 = u - PK_UNITS1;
  const u32 base = w1 ? (u32)WS_W1P + (4 * (u / 28) * 28 + u % 28) * 1024 : (u32)WS_WDF + (2 * (u2 / 56) * 112 + 2 * (u2 % 56)) * 1024;
  const u32 s1 = w1 ? 28 * 1024 : 112 * 1024, s2 = w1 ? 56 * 1024 : 1024;
#pragma unroll
  for (int q = 0; q < 4; ++q) bstore_a<16>(f[q], rs_ws, vo + base + (q & 1) * s1 + (q >> 1) * s2);
}
DI void pack_rd_st(rsrc_t rs_ws, const char* lds_wave, int u, u32 mask, int lane) {
  u32x4 f[4];
  pack_rd(lds_wave, u, lane, f);
  pack_st(rs_ws, u, mask, lane, f);
}
#define VM0() { asm volatile("" ::: "memory"); __builtin_amdgcn_s_waitcnt(0x0F70); asm volatile("" ::: "memory"); }
#define LBAR() asm volatile("s_waitcnt lgkmcnt(0)\n\ts_barrier" ::: "memory")
#define STAMPD(i)
#define STAMPE(i)
#define STAMPA(i)
#define STAMPB(i)
#define STAMPC(i)
#define G1A 4
#define NLO1 28
constexpr int B_KS = 2112, B_TT = 1056, B_HB = 528;
constexpr int B_CS = 28 * B_KS;
constexpr int B_ST = B_CS + 3584;
constexpr int B_SV = B_ST + 2048;
constexpr int S_W = B_SV + 2048;
#define FLS 1
constexpr int C_XP = FLS ? 328 : 320;
constexpr int C_X1 = 131072;
constexpr int C_GB = C_X1 + 32 * C_XP;
DI f32x16 mfma32(u32x4 a, s16x8 b, f32x16 c) { return __builtin_amdgcn_mfma_f32_32x32x16_bf16(__builtin_bit_cast(s16x8, a), b, c, 0, 0, 0); }
DI float dot2s(u32 a, u32 s, float c) { float d; asm("v_dot2_f32_bf16 %0, %1, %2, %3" : "=v"(d) : "v"(a), "s"(s), "v"(c)); return d; }
DI float fadd1(float a, float b) { float d; asm("v_add_f32 %0, %1, %2" : "=v"(d) : "v"(a), "v"(b)); return d; }
DI float fmul1(float a, float b) { float d; asm("v_mul_f32 %0, %1, %2" : "=v"(d) : "v"(a), "v"(b)); return d; }
#define PIN4(a) asm volatile("" : "+v"(a[0]), "+v"(a[1]), "+v"(a[2]), "+v"(a[3]))
#define S2POS 0
#define RPOS 0
#define PRO2 0
#define MG2 0
#define HMAP 0
#define PKS 128
#define NPRO 4
#define NPF_LO 28
#define NDM 17
#define NLO0 28
#define NHI0 18
#define WD0 0
#define XREST 2
static_assert(NPRO < 7 && WD0 <= 22, "");
#define HWIN 12
constexpr int hl_done(int g) {
  constexpr int tot = (7 - NPRO) * 18, first = tot - 18;
  return g < 0 ? 0 : g <= 26 ? (first * (g + 1) + 26) / 27 : g <= 42 ? first + (18 * (g - 26) + 15) / 16 : tot;
}
constexpr int kstep(int n) { return 7 * (n & 3) + (n >> 2); }
DI u32 w1_base(int nt, int wv, int second) {
  const int T = second ? 14 * nt + (wv < 4 ? 2 * wv + 1 : 12 + ((wv - 4) >> 1)) : 14 * nt + (wv < 4 ? 2 * wv : wv + 4);
  return (u32)WS_W1P + T * 28 * 1024;
}
DI u32x4 w1_load(rsrc_t rs_ws, u32 b1, u32 b2, int lane, int i) {
  const int s = kstep(i < 28 ? i : i - 28);
  return bload_sc1(rs_ws, lane * 16 + (s & 3) * 1024, (i < 28 ? b1 : b2) + (s >> 2) * 4096);
}

DI void dma1k(u32x4 desc, const char* lds, u32 voff, u32 soff) {
  asm volatile("s_mov_b32 m0, %0\n\ts_nop 0\n\tbuffer_load_dwordx4 %1, %2, %3 offen sc1 lds"
               :: "s"((u32)(size_t)(const LDSP char*)lds), "v"(voff), "s"(desc), "s"(soff) : "memory");
}
DI u32 flag_load(rsrc_t rs_ws, u32 off) { return __builtin_amdgcn_raw_buffer_load_b32(rs_ws, off, 0, 16); }
DI void flag_wait(rsrc_t rs_ws, u32 off, u32 v, u32 tag) {
#pragma nounroll
  for (u32 spins = 0; spins < SPIN_CAP; ++spins) {
    if (__all(v == tag)) break;
    __builtin_amdgcn_s_sleep(2);
    v = flag_load(rs_ws, off);
  }
  asm volatile("" ::: "memory");
}
template <bool HI>
DI void phase_BC(char* __restrict__ smem, char* __restrict__ ws, u16* __restrict__ out, const rsrc_t rs_ws, const u32 tag, const int mt, const int nt,
                 const int mt2, const int nt2, const int wv, const int lane, const int tid, const u32 touched, u32x4 (&w)[56]
) {
  constexpr int NPF = HI ? 56 : NPF_LO;
  constexpr int NWDP = NWDA + 28 - WD0 < 28 ? NWDA + 28 - WD0 : 28;
  const u32 wb1 = w1_base(nt, wv, 0), wb2 = w1_base(nt, wv, 1);
  const int nt2c = nt2 < 7 ? nt2 : 6;
  const u32 wdb = (u32)WS_WDF + ((2 * nt2c) * 112 + 14 * wv) * 1024;
  u32x4 wd[28];
  auto ldwd = [&](int i) { wd[i] = bload_sc1(rs_ws, lane * 16 + ((i % 14) & 3) * 1024, wdb + (i / 14) * 112 * 1024 + ((i % 14) >> 2) * 4096); };
  auto stage2 = [&]() {
    const u32x4 ws_desc = {(u32)(u64)ws, (u32)((u64)ws >> 32) & 0xffffu, (u32)WS_END, 0x00020000u};
    const u32 wt2 = (u32)WS_W1P + (14 * nt + 12) * 28 * 1024;
#pragma unroll
    for (int i = 0; i < 7; ++i) dma1k(ws_desc, smem + S_W + (wv * 7 + i) * 1024, lane * 16, wt2 + (wv * 7 + i) * 1024);
  };
  const int sp = wv & 1, qd = wv >> 1;
  u32x4 xr[7];
  u32 hf_flag;
  const u32 hfo = HFW < 0 || wv == HFW ? (u32)WS_PKF + (196 + min(lane, 48)) * PKS : OOB;
  {
    u32x4 cb, sb;
    const u32 xo = (lane >> 3) * 256 + (lane & 7) * 16, xb = (u32)WS_X1G + (mt * 224 + 56 * qd) * 256 + sp * 128;
    const u32 mo = (tid < 224 ? tid : 0) * 16;
    auto ldx = [&](int n) { xr[n] = bload_sc1(rs_ws, xo, xb + n * 2048); };
    auto okx = [&](int n) { return __all(umx4(xr[n]) != X1_VOID); };
    if (!HI) {
#pragma unroll
      for (int i = 0; i < NDM; ++i) w[i] = *(const u32x4*)(smem + S_W + (wv * NDM + i) * 1024 + lane * 16);
    }
    LBAR();
    if (S2POS == 0) stage2();
    SB();
    cb = bload_sc1(rs_ws, mo, (u32)WS_MODG + 4 * D_ * 8); sb = bload_sc1(rs_ws, mo, (u32)WS_MODG + 3 * D_ * 8);
    hf_flag = flag_load(rs_ws, hfo);
#pragma unroll
    for (int n = 0; n < 7; ++n) ldx(n);
    SB();
    auto ldrest = [&]() {
#pragma unroll
      for (int i = HI ? NHI0 : NDM; i < NPF - (HI ? 28 : 0); ++i) w[i] = w1_load(rs_ws, wb1, wb2, lane, i);
    };
    if (RPOS == 1) { ldrest(); SB(); }
    if (S2POS == 1) { stage2(); SB(); }
    SB();
    STAMPB(3) STAMPC(9)
#pragma nounroll
    for (u32 spins = 0; spins < SPIN_CAP; ++spins) {
      u32 pend = 0;
#pragma unroll
      for (int n = 0; n < 7; ++n) pend |= okx(n) ? 0u : 1u << n;
      const bool okm = __all(cb[1] == tag && cb[3] == tag && sb[1] == tag && sb[3] == tag);
      if (__builtin_expect(pend == 0 && okm, 1)) break;
      __builtin_amdgcn_s_sleep(2);
      if (pend == 0x7fu) pend = 1u;
#pragma unroll
      for (int n = 0; n < 7; ++n) if (pend >> n & 1) ldx(n);
      if (!okm) { cb = bload_sc1(rs_ws, mo, (u32)WS_MODG + 4 * D_ * 8); sb = bload_sc1(rs_ws, mo, (u32)WS_MODG + 3 * D_ * 8); }
    }
    if (S2POS == 2) { SB(); stage2(); SB(); }
    if (tid < 224) *(u32x4*)(smem + B_CS + tid * 16) = u32x4{cb[0], cb[2], sb[0], sb[2]};
  }
  KEEP(touched);
  STAMPB(4) STAMPC(10) STAMPD(2)
  float nmean[4], rs[4];
  u32 wdt = 0;
  auto wdt_load = [&]() { return __builtin_amdgcn_raw_buffer_load_b32(rs_ws, (((blockIdx.x >> 3) * 8 + wv) * 49 + min(lane, 48)) * 128, (u32)WS_WDF, 16); };
  {
    float sq[8] = {0, 0, 0, 0, 0, 0, 0, 0};
#pragma unroll
    for (int i = 0; i < 7; ++i) {
#pragma unroll
      for (int j = 0; j < 4; ++j) { sq[j] = dot2bf(xr[i][j], 0x3f803f80u, sq[j]); sq[4 + j] = dot2bf(xr[i][j], xr[i][j], sq[4 + j]); }
    }
    float r4[4], r2[2];
#pragma unroll
    for (int j = 0; j < 4; ++j) { auto t = __builtin_amdgcn_permlane32_swap(f2u(sq[j]), f2u(sq[4 + j]), false, false); r4[j] = u2f(t[0]) + u2f(t[1]); }
#pragma unroll
    for (int j = 0; j < 2; ++j) { auto t = __builtin_amdgcn_permlane16_swap(f2u(r4[2 * j]), f2u(r4[2 * j + 1]), false, false); r2[j] = dpp_add<0x128>(u2f(t[0]) + u2f(t[1])); }
    const int rho = lane >> 4, jj = (lane >> 3) & 1;
    ((float*)(smem + B_ST))[(wv * 8 + (lane & 7)) * 8 + (rho >> 1) * 4 + 2 * jj + (rho & 1)] = jj ? r2[1] : r2[0];
    STAMPD(3)
    if (HFW < 0 || wv == HFW) flag_wait(rs_ws, hfo, hf_flag, tag);
    STAMPD(4)
    LBAR();
    STAMPD(5)
    if (HI && WDA_POS == 0) {
#pragma unroll
      for (int i = 0; i < NWDA; ++i) ldwd(i);
    }
    STAMPD(6)
    const int t4 = (lane >> 3) & 3;
    const float* ps = (const float*)(smem + B_ST) + (sp * 8 + (lane & 7)) * 8 + t4;
    float s = 0.f, q = 0.f;
#pragma unroll
    for (int k = 0; k < 4; ++k) { s += ps[k * 128]; q += ps[k * 128 + 4]; }
    const float m = s * (1.f / D_), var = fmaxf(q * (1.f / D_) - m * m, 0.f);
    float* sv = (float*)(smem + B_SV) + (wv * 8 + (lane & 7)) * 8;
    sv[t4] = -rbf(m); sv[4 + t4] = __builtin_amdgcn_rcpf(rbf(__builtin_amdgcn_sqrtf(rbf(rbf(var) + 1e-5f))));
    __builtin_amdgcn_wave_barrier();
    const f32x4 a = *(const f32x4*)sv, c = *(const f32x4*)(sv + 4);
#pragma unroll
    for (int j = 0; j < 4; ++j) { nmean[j] = a[j]; rs[j] = c[j]; }
  }
  STAMPD(7)
  char* hw = smem + 7 * qd * B_KS + sp * B_TT + (lane >> 5) * B_HB + (lane & 7) * 32 + ((lane >> 3) & 3) * 4;
  const char* csp = smem + B_CS + (56 * qd + (lane >> 3)) * 16;
  {
    const u32 sel_lo = 0x00003f80u, sel_hi = 0x3f800000u;
    f32x2 v[4], tv[4], k1, k2; f32x4 cs;
    if (HI && PRIO_P > 0) __builtin_amdgcn_s_setprio(1);
#pragma unroll
    for (int i = 0; i < NPRO; ++i) {
      if (HI && PRIO_P > 0 && i == PRIO_P) __builtin_amdgcn_s_setprio(0);
      cs = *(const f32x4*)(csp + i * 128);
#pragma unroll
      for (int j = 0; j < 4; ++j) v[j] = f32x2{dot2s(xr[i][j], sel_lo, nmean[j]), dot2s(xr[i][j], sel_hi, nmean[j])};
      { f32x2 c = {cs[0], cs[1]}; k1 = c * 65537.f; k2 = c * -65536.f; }
#pragma unroll
      for (int j = 0; j < 4; ++j) v[j] = f32x2{rbf(v[j][0]), rbf(v[j][1])} * rs[j];
#pragma unroll
      for (int j = 0; j < 4; ++j) v[j] = f32x2{rbf(v[j][0]), rbf(v[j][1])};
#pragma unroll
      for (int j = 0; j < 4; ++j) tv[j] = v[j] * k1;
#pragma unroll
      for (int j = 0; j < 4; ++j) v[j] = __builtin_elementwise_fma(v[j], k2, tv[j]) + f32x2{cs[2], cs[3]};
#pragma unroll
      for (int j = 0; j < 4; ++j) *(u32*)(hw + i * B_KS + (j >> 1) * 256 + (j & 1) * 16) = pk_bf16(v[j][0], v[j][1]);
      SB();
      if (i == 0) { STAMPD(8) }
      if (WDT_POS == 2 + i) { wdt = wdt_load(); SB(); }
      if (HI && WDA_POS == 2) {
#pragma unroll
        for (int k = (NWDA * i) / NPRO; k < (NWDA * (i + 1)) / NPRO; ++k) ldwd(k);
        SB();
      }
    }
  }
  if (HI && PRIO_P >= NPRO) __builtin_amdgcn_s_setprio(0);
  if (S2POS >= 1) asm volatile("s_waitcnt vmcnt(0)" ::: "memory");
  if (WDT_POS == 1) { wdt = wdt_load(); SB(); }
  if (HI && WDA_POS == 1) {
#pragma unroll
    for (int i = 0; i < NWDA; ++i) ldwd(i);
    SB();
  }
  {
    const int mt2_ = mt2, nt2_ = nt2, tk = 8 * (wv & 3) + (lane >> 3);
    const rsrc_t rs_out = __builtin_amdgcn_make_buffer_rsrc(out, 0, L_ * D_ * 2, 0x00020000);
    const u32 row = 128 * (mt2_ >> 1) + 16 * ((tk >> 1) & 7) + 8 * (mt2_ & 1) + 4 * (wv >> 2) + 2 * (tk >> 4) + (tk & 1);
    bstore_a<OW_AUX>(u32x4{0u, 0u, 0u, 0u}, rs_out, (lane & 7) == 0 && nt2_ < 7 ? row * (D_ * 2) + nt2_ * 128 : OOB);
  }
  STAMPB(5) STAMPD(9)
  LBAR();
  STAMPD(10)
  const char* hr = smem + (lane >> 5) * B_HB + (lane & 31) * 16;
  auto hrd = [&](int ks, int tt) { return *(const s16x8*)(hr + ks * B_KS + tt * B_TT); };
  float v[4][2]; f32x4 cs;
  auto nhalf = [&](int h) {
    const int i = NPRO + h / 18, lev = (h % 18) >> 1, hf = h & 1;
    if (lev == 0 && hf == 0) cs = *(const f32x4*)(csp + i * 128);
#pragma unroll
    for (int j = 2 * hf; j < 2 * hf + 2; ++j) {
      if (lev == 0) { v[j][0] = bflo(xr[i][j]); v[j][1] = bfhi(xr[i][j]); }
#pragma unroll
      for (int c = 0; c < 2; ++c) {
        if (lev == 1) v[j][c] = fadd1(v[j][c], nmean[j]);
        if (lev == 2 || lev == 4 || lev == 6) v[j][c] = rbf(v[j][c]);
        if (lev == 3) v[j][c] = fmul1(v[j][c], rs[j]);
        if (lev == 5) v[j][c] = fmul1(v[j][c], cs[c]);
        if (lev == 7) v[j][c] = fadd1(v[j][c], cs[2 + c]);
      }
      if (lev == 8) *(u32*)(hw + i * B_KS + (j >> 1) * 256 + (j & 1) * 16) = pk_bf16(v[j][0], v[j][1]);
    }
    if (lev < 8) asm volatile("" : "+v"(v[2 * hf][0]), "+v"(v[2 * hf][1]), "+v"(v[2 * hf + 1][0]), "+v"(v[2 * hf + 1][1]));
  };
  f32x16 a0[2] = {};
  {
    s16x8 hf[28][2];
#pragma unroll
    for (int q = 0; q < 2; ++q) { hf[q][0] = hrd(kstep(q), 0); hf[q][1] = hrd(kstep(q), 1); }
    SB();
    static_for<56>([&](auto G) {
      constexpr int g = G, ks = g >> 1, tt = g & 1;
      a0[tt] = mfma32(w[ks], hf[ks][tt], a0[tt]);
      SB();
      if (ks + 2 < 28) hf[ks + 2 < 28 ? ks + 2 : 0][tt] = hrd(kstep(ks + 2), tt);
      static_for<hl_done(g) - hl_done(g - 1)>([&](auto Hh) { nhalf(hl_done(g - 1) + Hh); });
      if (tt == 1 && NPF + ks < 56) w[NPF + ks < 56 ? NPF + ks : 0] = w1_load(rs_ws, wb1, wb2, lane, NPF + ks);
      if (HI && tt == 1 && ks >= WD0 && NWDA + ks - WD0 < NWDP) ldwd(ks >= WD0 && NWDA + ks - WD0 < NWDP ? NWDA + ks - WD0 : 0);
      if (g >= 1) { KEEP(hf[(g >= 1 ? g - 1 : 0) >> 1][(g + 1) & 1]); }
      if (tt == 0 && ks >= 1) KEEP(w[ks >= 1 ? ks - 1 : 0]);
      SB();
      if (XREST != 2 && g == 43) {
        constexpr int younger = 1 + (HI ? NWDA + (22 - WD0 < NWDP - NWDA ? 22 - WD0 : NWDP - NWDA) : (NPF + 22 <= 56 ? 22 : 56 - NPF));
        asm volatile("s_waitcnt vmcnt(%0)" :: "n"(younger) : "memory");
      }
      if (g == 27 || g == 43) { LBAR(); SB(); }
    });
    KEEP(hf[27][1]); KEEP(w[27]); KEEP(wdt);
    asm volatile("" : "+v"(a0[0]), "+v"(a0[1]));
  }
  STAMPB(6)
  const u32 hbase = (u32)WS_HHF + (2 * mt) * 112 * 1024 + lane * 16;
  const u32 T1o = (wb1 - (u32)WS_W1P) / 28, T2o = (wb2 - (u32)WS_W1P) / 28;
  const u32 x1o = (u32)WS_X1G + (mt2 * 224 + 32 * nt2c) * 256 + wv * 1024, x1v = lane * 16, gbo = (u32)WS_MODG + (5 * D_ + 64 * nt2c) * 8;
  u32x4 x1t = bload_sc1(rs_ws, x1v, x1o);
  u32x2 gbv = {0u, tag};
  if (!HI) gbv = __builtin_bit_cast(u32x2, __builtin_amdgcn_raw_buffer_load_b64(rs_ws, lane * 8, gbo, 16));
  float g8[8], u8[8];
  auto ehalf = [&](const f32x16& acc, int h, u32 dst) {
    const int lev = h >> 1, ap = h & 1;
#pragma unroll
    for (int b = 0; b < 4; ++b) {
      const int k = 4 * ap + b;
      if (lev == 0) g8[k] = rbf(acc[8 * ap + b]);
      if (lev == 1) u8[k] = rbf(acc[8 * ap + 4 + b]);
      if (lev == 2) g8[k] = fmul1(g8[k], -1.44269504088896341f);
      if (lev == 3) g8[k] = __builtin_amdgcn_exp2f(g8[k]);
      if (lev == 4) g8[k] += 1.f;
      if (lev == 5) g8[k] = __builtin_amdgcn_rcpf(g8[k]);
      if (lev == 6) g8[k] = rbf(g8[k]);
      if (lev == 7) g8[k] = fmul1(g8[k], u8[k]);
    }
    if (lev < 8) asm volatile("" : "+v"(g8[4 * ap]), "+v"(g8[4 * ap + 1]), "+v"(g8[4 * ap + 2]), "+v"(g8[4 * ap + 3]));
    if (lev == 8 && ap == 1)
      bstore_a<16>(u32x4{pk_bf16(g8[0], g8[1]), pk_bf16(g8[2], g8[3]), pk_bf16(g8[4], g8[5]), pk_bf16(g8[6], g8[7])}, rs_ws, dst);
  };
  if (!HI) {
    f32x16 a1[2] = {};
    s16x8 hf[56];
#pragma unroll
    for (int q = 0; q < 3; ++q) hf[q] = hrd(kstep(q), 0);
    SB();
    static_for<56>([&](auto G) {
      constexpr int g = G, tt = g / 28, ks = g % 28;
      a1[tt] = mfma32(w[28 + ks], hf[g], a1[tt]);
      SB();
      if (g + 3 < 56) hf[g + 3 < 56 ? g + 3 : 0] = hrd(kstep((g + 3) % 28), (g + 3) / 28);
      constexpr int e0 = g < 28 ? (36 * g + 27) / 28 : 36 + (18 * (g - 28) + 27) / 28, e1 = g + 1 <= 28 ? (36 * (g + 1) + 27) / 28 : 36 + (18 * (g + 1 - 28) + 27) / 28;
      static_for<e1 - e0>([&](auto Hh) {
        constexpr int h = e0 + Hh, t = h / 18;
        if (t == 0) ehalf(a0[0], h % 18, hbase + T1o);
        if (t == 1) ehalf(a0[1], h % 18, hbase + 112 * 1024 + T1o);
        if (t == 2) ehalf(a1[0], h % 18, hbase + T2o);
      });
      if (tt == 1) ldwd(ks);
      if (g >= 1) KEEP(hf[g >= 1 ? g - 1 : 0]);
      if (g >= 29) KEEP(w[28 + (g >= 29 ? g - 29 : 0)]);
      SB();
    });
    KEEP(hf[55]); KEEP(w[55]);
    STAMPB(7)
#pragma unroll
    for (int h = 0; h < 18; ++h) { ehalf(a1[1], h, hbase + 112 * 1024 + T2o); SB(); }
  } else {
    const int tt2 = wv & 1;
    const char* hr2 = hr + tt2 * B_TT;
    const char* wr2 = smem + S_W + ((wv - 4) >> 1) * 28 * 1024 + lane * 16;
    f32x16 a1 = {};
    s16x8 hf[28]; u32x4 w2[28];
#pragma unroll
    for (int q = 0; q < 3; ++q) { w2[q] = *(const u32x4*)(wr2 + kstep(q) * 1024); hf[q] = *(const s16x8*)(hr2 + kstep(q) * B_KS); }
    SB();
    static_for<28>([&](auto G) {
      constexpr int g = G;
      a1 = mfma32(w2[g], hf[g], a1);
      SB();
      if (g + 3 < 28) { w2[g + 3 < 28 ? g + 3 : 0] = *(const u32x4*)(wr2 + kstep(g + 3) * 1024); hf[g + 3 < 28 ? g + 3 : 0] = *(const s16x8*)(hr2 + kstep(g + 3) * B_KS); }
      constexpr int e0 = (36 * g + 27) / 28, e1 = (36 * (g + 1) + 27) / 28;
      static_for<e1 - e0>([&](auto Hh) {
        constexpr int h = e0 + Hh, t = h / 18;
        if (t == 0) ehalf(a0[0], h % 18, hbase + T1o);
        if (t == 1) ehalf(a0[1], h % 18, hbase + 112 * 1024 + T1o);
      });
      if (NWDP + g < 28) ldwd(NWDP + g < 28 ? NWDP + g : 0);
      if (g >= 1) { KEEP(hf[g >= 1 ? g - 1 : 0]); KEEP(w2[g >= 1 ? g - 1 : 0]); }
      SB();
    });
    KEEP(hf[27]); KEEP(w2[27]);
    STAMPB(7)
#pragma unroll
    for (int h = 0; h < 18; ++h) { ehalf(a1, h, hbase + tt2 * 112 * 1024 + T2o); SB(); }
  }
  asm volatile("s_waitcnt vmcnt(0)" ::: "memory");
#pragma nounroll
  for (u32 spins = 0; spins < SPIN_CAP; ++spins) {
    if (__all(umx4(x1t) != X1_VOID && gbv[1] == tag)) break;
    __builtin_amdgcn_s_sleep(1);
    x1t = bload_sc1(rs_ws, x1v, x1o);
    if (!HI) gbv = __builtin_bit_cast(u32x2, __builtin_amdgcn_raw_buffer_load_b64(rs_ws, lane * 8, gbo, 16));
  }
  { char* xp = smem + C_X1 + (4 * wv + (lane >> 4)) * C_XP + ((lane >> 3) & 1) * 128 + (lane & 7) * 8;
    *(u32x2*)xp = u32x2{x1t[0], x1t[1]}; *(u32x2*)(xp + 64) = u32x2{x1t[2], x1t[3]}; }
  if (!HI && wv == 0) ((u32*)(smem + C_GB))[lane] = gbv[0];
  LBAR();
  if (tid == 0) __hip_atomic_store((gu32*)(ws + WS_HFLG + (mt * 8 + nt) * 128), tag, RLX_AGENT);
  STAMPB(8)
  if (nt2 == 7) return;
  {
    const gu32* fl = (const gu32*)(ws + WS_HFLG + (mt2 * 8 + wv) * 128);
#pragma nounroll
    for (u32 spins = 0; spins < SPIN_CAP; ++spins) {
      if (__builtin_amdgcn_readfirstlane(__hip_atomic_load(fl, RLX_AGENT)) == tag) break;
      __builtin_amdgcn_s_sleep(1);
    }
    asm volatile("" ::: "memory");
  }
  STAMPB(9)
  const u32 hab = (u32)WS_HHF + ((2 * mt2) * 112 + 14 * wv) * 1024;
  u32x4 hh[28];
  auto ldhh = [&](int g) { hh[g] = bload_sc1(rs_ws, lane * 16 + ((g % 14) & 3) * 1024, hab + (g / 14) * 112 * 1024 + ((g % 14) >> 2) * 4096); };
#pragma unroll
  for (int g = 0; g < HWIN; ++g) ldhh(g);
  SB();
  f32x16 acc[2][2] = {};
  auto red_wr = [&](int tile, int q) {
    const f32x16& a = acc[tile >> 1][tile & 1];
    *(f32x4*)(smem + (tile * 8 + wv) * 4096 + q * 1024 + (FLS ? lane ^ (q << 2) : lane) * 16) = f32x4{a[4 * q], a[4 * q + 1], a[4 * q + 2], a[4 * q + 3]};
  };
  static_for<28>([&](auto G) {
    constexpr int g = G, tt = g / 14, s = g % 14;
    acc[tt][0] = mfma32(wd[s], __builtin_bit_cast(s16x8, hh[g]), acc[tt][0]);
    SB();
    if (g + HWIN < 28) ldhh(g + HWIN < 28 ? g + HWIN : 0);
    if (g >= 1) KEEP(hh[g >= 1 ? g - 1 : 0]);
    SB();
    acc[tt][1] = mfma32(wd[14 + s], __builtin_bit_cast(s16x8, hh[g]), acc[tt][1]);
    SB();
    if (g >= 16 && g < 24) red_wr((g - 16) >> 2 & 1, (g - 16) & 3);
    SB();
  });
  KEEP(hh[27]);
  STAMPB(10)
#pragma unroll
  for (int q = 0; q < 8; ++q) red_wr(2 + (q >> 2), q & 3);
  LBAR();
  {
    const int tt = wv >> 2, tk = 8 * (wv & 3) + (lane >> 3), c8 = lane & 7, dt = c8 >> 2, q = c8 & 3;
    u32 o[4];
#pragma unroll
    for (int hb = 0; hb < 2; ++hb) {
      const char* rp = smem + (2 * tt + dt) * 8 * 4096 + q * 1024 + ((32 * hb + tk) ^ (q << 2)) * 16;
      f32x4 p[8];
#pragma unroll
      for (int sl = 0; sl < 8; ++sl) p[sl] = *(const f32x4*)(rp + sl * 4096);
      f32x4 v = ((p[0] + p[1]) + (p[2] + p[3])) + ((p[4] + p[5]) + (p[6] + p[7]));
      f32x4 gb = *(const f32x4*)(smem + C_GB + (8 * c8 + 4 * hb) * 4);
      const char* xp = smem + C_X1 + (4 * c8 + 2 * hb) * C_XP + tt * 128 + tk * 4;
      u32 xa = *(const u32*)xp, xb = *(const u32*)(xp + C_XP);
      float r0 = bflo(xa) + rbf(gb[0] * rbf(v[0])), r1 = bfhi(xa) + rbf(gb[1] * rbf(v[1]));
      float r2 = bflo(xb) + rbf(gb[2] * rbf(v[2])), r3 = bfhi(xb) + rbf(gb[3] * rbf(v[3]));
      o[2 * hb] = pk_bf16(r0, r1); o[2 * hb + 1] = pk_bf16(r2, r3);
    }
    const rsrc_t rs_out = __builtin_amdgcn_make_buffer_rsrc(out, 0, L_ * D_ * 2, 0x00020000);
    const u32 row = 128 * (mt2 >> 1) + 16 * ((tk >> 1) & 7) + 8 * (mt2 & 1) + 4 * tt + 2 * (tk >> 4) + (tk & 1);
    bstore_a<OUT_AUX>(u32x4{o[0], o[1], o[2], o[3]}, rs_out, row * (D_ * 2) + nt2 * 128 + c8 * 16);
  }
}
constexpr int TROW = 272, TWAVE = 64 * TROW;
constexpr int A_H   = 8 * TWAVE;
constexpr int A_SW  = A_H + 9216;
constexpr int A_XT  = A_SW + 128;
constexpr int A_KD  = A_XT;
constexpr int A_XS  = A_KD + 6144;
constexpr int A_LDS = A_XT + 16 * 912;
static_assert(A_XS + 8192 <= A_LDS, "");
constexpr int CS    = 4160;
constexpr int A_KC  = 0;
constexpr int A_RB  = 33280;

constexpr u32 KZERO = 1410 * 2, KDUMMY = 1411 * 2, KFIRST = 1412 * 2;
constexpr u32 lag_off(int l) {
  if (l < 0) return KZERO;
  if (l == 0) return KFIRST;
  int e = 0;
  while ((l >> (e + 1)) != 0) ++e;
  int q = 0;
  if (e <= 7) q = l << (7 - e);
  else {
    int sh = e - 7, rem = l & ((1 << sh) - 1), half = 1 << (sh - 1);
    q = l >> sh;
    if (rem > half || (rem == half && (q & 1))) ++q;
    if (q == 256) { q = 128; ++e; }
  }
  return (u32)((e << 7) + (q - 128)) * 2;
}
struct IdxTab { u32 v[256 * 8]; };
constexpr IdxTab make_idx_tab() {
  IdxTab t{};
  for (int q = 0; q < 256; ++q) {
    for (int e = 0; e < 6; ++e) t.v[q * 8 + e] = lag_off(8 * q - 3 + 2 * e) | (lag_off(8 * q - 2 + 2 * e) << 16);
    int rho0 = 16 * (q >> 6) + 4 * ((q >> 4) & 3), b = q & 15;
    int sh = (rho0 >> 3) - 1; sh = sh < 0 ? 0 : (sh > 3 ? 3 : sh);
    u32 o[4] = {0, 0, 0, 0};
    for (int r = 0; r < 4; ++r) {
      int m = (rho0 - 8 * sh + r) << sh, l = ((m >> 3) << 7) + 8 * b + (m & 7);
      o[r] = rho0 < 40 ? lag_off(l) : ((rho0 == 40 && b == 0 && r == 0) ? lag_off(2048) : KDUMMY);
    }
    t.v[q * 8 + 6] = o[0] | (o[1] << 16); t.v[q * 8 + 7] = o[2] | (o[3] << 16);
  }
  return t;
}
static __device__ const IdxTab idx_tab __attribute__((aligned(128))) = make_idx_tab();

#define TEXT_LINES 313
DI u32 code_prefetch(u64 base, int line) { return *(const GAS u32*)(base + (u64)min(line, TEXT_LINES - 1) * 128); }

#define PL_POS 0
#define PK_POS 0
#define PUB_POS 4
#define NCV 0
#define VOID_POS 3
#define ARRL 1
#define VPOS 24
#define XLO 0
#define XLO0 4
static_assert(XLO0 >= 4 && XLO0 <= 7, "");
#define XS0 2
#define XMAP 0
#define TLBW 0
#define TOUCH 0
#define PK_SPLIT 1
#define XPOS 0
#define MG_EARLY 1
#define PKW 0
#define PUBB 6
#define HDEF 1
#define PEEL 1
#define PACKER (wv < 4)
#define CAS __attribute__((address_space(4)))
DI void bstore8_sc1(u32x2 v, rsrc_t r, u32 off) { __builtin_amdgcn_raw_buffer_store_b64(__builtin_bit_cast(__attribute__((ext_vector_type(2))) int, v), r, off, 0, 16); }
__global__ __launch_bounds__(512, 2) void k_all(
    const u16* __restrict__ x, const u16* __restrict__ Are, const u16* __restrict__ Aim, const u16* __restrict__ Bre,
    const u16* __restrict__ Bim, const u16* __restrict__ Cre, const u16* __restrict__ Cim, const u16* __restrict__ logdt,
    const u16* __restrict__ dskip, const u16* __restrict__ cvec, const u16* __restrict__ wmod,
    const u16* __restrict__ wgate, const u16* __restrict__ wup, const u16* __restrict__ wdown, u16* __restrict__ out, char* __restrict__ ws) {
  __shared__ __attribute__((aligned(16))) char smem[A_LDS];
  const u64 pc0 = __builtin_amdgcn_s_getpc() & ~(u64)127;
  STAMP_DECL
  STAMP_RT(0) STAMP(1)
  asm volatile("" :: "s"(x), "s"(Are), "s"(Aim), "s"(Bre), "s"(Bim), "s"(Cre), "s"(Cim), "s"(logdt), "s"(dskip), "s"(cvec), "s"(wmod),
               "s"(wgate), "s"(wup), "s"(wdown), "s"(out), "s"(ws));
  const int tid = threadIdx.x, lane = tid & 63, wv = __builtin_amdgcn_readfirstlane(tid >> 6);
  const int xcd = blockIdx.x & 7, idx = blockIdx.x >> 3;
  const rsrc_t rs_ws = __builtin_amdgcn_make_buffer_rsrc(ws, 0, (int)WS_END, 0x00020000);
  const u32 E = __hip_atomic_load((gu32*)(ws + WS_EPOCH), RLX_AGENT);
  u32x4 pv[4] = {};
  u32 tag, x1off = OOB, xv[4] = {0, 0, 0, 0};
  u32 hdp[4] = {0, 0, 0, 0};
  float ga[2] = {0.f, 0.f}, post[2] = {0.f, 0.f};
  const int ci = idx - 4, cw = xcd * 28 + ci;
  const int xrank = xcd < 4 ? xcd : xcd >= 6 ? xcd - 2 : xcd + 2;
  auto publish_pack = [&]() {
    if (PACKER) {
      VM0();
      if (lane == 0) {
        const u32 n = __hip_atomic_fetch_add((LDSP u32*)(smem + A_SW + 64), 1u, __ATOMIC_RELAXED, __HIP_MEMORY_SCOPE_WORKGROUP);
        if (n == 3) __hip_atomic_store((gu32*)(ws + WS_PKF + cw * PKS), tag, RLX_AGENT);
      }
    }
  };
  if (idx < 4) {
    const int cu = xcd + 8 * idx;
    const bool crit = cu < 21;
    const int nk = idx == 3 ? xcd : 3 + xcd;
    const int un0 = crit ? cu : 21 + 2 * nk, nun = (crit || un0 == 41) ? 1 : 2;
    if (tid == 0) *(u32x4*)(smem + A_SW + 64) = u32x4{0u, 0u, 0u, 0u};
    u16 cv = cvec[tid < D_ ? tid : 0];
    u32x4 wm[7];
    auto ldwm = [&](int un) {
#pragma unroll
      for (int j = 0; j < 7; ++j) wm[j] = *(const u32x4*)(wmod + (size_t)(8 * (wv + 8 * j) + (lane >> 3)) * (6 * D_) + 64 * un + 8 * (lane & 7));
    };
    ldwm(un0);
    SB();
    tag = __builtin_amdgcn_readfirstlane(E) + TAG_C;
    if (tid == 0) __hip_atomic_store((gu64*)(ws + WS_ARR) + blockIdx.x, ((u64)tag << 32) | tag, RLX_AGENT);
    { float c = bf2f(cv); float sl = rbf(c / (1.f + expf(-c))); if (tid < D_) ((float*)smem)[tid] = sl; }
    STAMPA(2)
    __syncthreads();
    float* red = (float*)(smem + 2048);
    for (int uu = 0; uu < nun; ++uu) {
      float acc[8] = {0, 0, 0, 0, 0, 0, 0, 0};
#pragma unroll
      for (int j = 0; j < 7; ++j) {
        float sl = ((const float*)smem)[8 * (wv + 8 * j) + (lane >> 3)];
#pragma unroll
        for (int e = 0; e < 4; ++e) { acc[2 * e] = fmaf(sl, bflo(wm[j][e]), acc[2 * e]); acc[2 * e + 1] = fmaf(sl, bfhi(wm[j][e]), acc[2 * e + 1]); }
      }
      if (uu + 1 < nun) ldwm(un0 + 1);
      float r4[4], r2[2];
#pragma unroll
      for (int j = 0; j < 4; ++j) { auto t = __builtin_amdgcn_permlane32_swap(f2u(acc[j]), f2u(acc[4 + j]), false, false); r4[j] = u2f(t[0]) + u2f(t[1]); }
#pragma unroll
      for (int j = 0; j < 2; ++j) { auto t = __builtin_amdgcn_permlane16_swap(f2u(r4[j]), f2u(r4[j + 2]), false, false); r2[j] = dpp_add<0x128>(u2f(t[0]) + u2f(t[1])); }
      if (!(lane & 8)) {
        int rho = lane >> 4;
        *(f32x2*)(red + (uu * 8 + wv) * 64 + 8 * (lane & 7) + 2 * (rho & 1) + 4 * (rho >> 1)) = f32x2{r2[0], r2[1]};
      }
      STAMPA(3)
    }
    __syncthreads();
    if (tid < 64 * nun) {
      int uu = tid >> 6, cl = tid & 63, col = 64 * (un0 + uu) + cl;
      float s = 0.f;
#pragma unroll
      for (int w = 0; w < 8; ++w) s += red[(uu * 8 + w) * 64 + cl];
      float mv = rbf(s);
      const bool scale = (col >= D_ && col < 2 * D_) || (col >= 4 * D_ && col < 5 * D_);
      __hip_atomic_store((gu64*)(ws + WS_MODG) + col, ((u64)tag << 32) | f2u(scale ? rbf(1.f + mv) : mv), RLX_AGENT);
    }
    STAMPA(5)
    if (crit) {
      const int pu = 896 + cu * 8 + wv;
      const u32 m2 = pu + 168 < PK_UNITS ? 0u : OOB;
      u32x4 pv[2][4];
      pack_load2(wgate, wup, wdown, pu, 0u, lane, pv[0]);
      pack_load2(wgate, wup, wdown, min(pu + 168, PK_UNITS - 1), m2, lane, pv[1]);
      pack_wr(smem + 8192 + wv * 4096, lane, pv[0]);
      pack_rd(smem + 8192 + wv * 4096, pu, lane, pv[0]);
      pack_wr(smem + 8192 + wv * 4096, lane, pv[1]);
      pack_rd(smem + 8192 + wv * 4096, min(pu + 168, PK_UNITS - 1), lane, pv[1]);
      {
        const int nt_ = (xcd % (8 / G1A)) * G1A + idx % G1A;
        const u32 fo = (u32)WS_PKF + (7 * ((14 * nt_ >> 2) + ((lane >> 3) & 3)) + min(lane & 7, 6)) * PKS;
        if (wv == 0) flag_wait(rs_ws, fo, flag_load(rs_ws, fo), tag);
        LBAR();
      }
      pack_st(rs_ws, pu, 0u, lane, pv[0]);
      pack_st(rs_ws, min(pu + 168, PK_UNITS - 1), m2, lane, pv[1]);
      asm volatile("s_waitcnt vmcnt(0)" ::: "memory");
      LBAR();
      if (tid == 0) __hip_atomic_store((gu32*)(ws + WS_PKF + (224 + cu) * PKS), tag, RLX_AGENT);
    }
  } else {
  const int d0 = 2 * cw, ch = wv >> 2;
  const int P = xcd * 16 + ci;
  const u32 pmask = ci < 16 ? 0u : OOB;
  const int t0 = 256 * wv + 16 * (lane & 15) + 4 * (lane >> 4);
  x1off = (u32)WS_X1G + (((4 * wv + 2 * ((lane >> 3) & 1) + (lane >> 5)) * 224 + cw) * 2 + ((lane >> 4) & 1)) * 128 + (lane & 7) * 16;
  const u32 ld2 = *(const volatile CAS u32*)(logdt + d0), ds2 = *(const volatile CAS u32*)(dskip + d0);
  SB();
  u32x4 xr0, xr1, prm;
  {
    const rsrc_t rs_x = __builtin_amdgcn_make_buffer_rsrc((void*)x, 0, L_ * D_ * 2, 0x00020000);
    const int row = 256 * (P >> 4) + 16 * (8 * (P & 1) + (wv >> 1)) + 4 * ((P >> 1) & 3) + 2 * ((P >> 3) & 1) + (wv & 1);
    const u32 xo = (lane < 56 ? (u32)lane * 16 : OOB) | pmask;
    xr0 = bload(rs_x, xo, (row & 2047) * (D_ * 2)); xr1 = bload(rs_x, xo, ((row + 64) & 2047) * (D_ * 2));
    const int a = lane >> 3;
    u64 arr = (u64)Are;
    arr = a == 1 ? (u64)Aim : arr; arr = a == 2 ? (u64)Bre : arr; arr = a == 3 ? (u64)Bim : arr; arr = a == 4 ? (u64)Cre : arr; arr = a >= 5 ? (u64)Cim : arr;
    prm = *(const GAS u32x4*)(arr + (u64)((cw * 512 + wv * 64 + (lane & 7) * 8) * 2));
  }
  SB();
  u32 pf;
  {
    int n = wv * 6 + min(lane, 5), un = xcd + 8 * (n >> 4);
    u64 aw = (u64)(wmod + (size_t)(16 * ci + (n & 15)) * (6 * D_) + 64 * (un < 21 ? un : xcd));
    u64 ac = pc0 + (u64)min(ci * 13 + min(2 * wv + min(lane, 7) - 6, 12), TEXT_LINES - 1) * 128;
#define NPFL 6
    pf = *(const GAS u32*)(lane < NPFL ? aw : ac);
  }
  SB();
  tag = __builtin_amdgcn_readfirstlane(E) + TAG_C;
  u32 gx[4][2], xgoff;
  auto xg_st = [&](int i) { bstore_a<16>(u32x4{gx[i][0], tag, gx[i][1], tag}, rs_ws, (xgoff + i * (64 * 16384)) | (i == 3 && wv >= 4 ? OOB : 0u)); };
  {
    if (tid == 0) *(u32x4*)(smem + A_SW + 64) = u32x4{0u, 0u, 0u, 0u};
    if (lane < 57) { *(u32x4*)(smem + A_XT + wv * 912 + lane * 16) = xr0; *(u32x4*)(smem + A_XT + (8 + wv) * 912 + lane * 16) = xr1; }
    float s0 = 0.f, q0 = 0.f, s1 = 0.f, q1 = 0.f;
#pragma unroll
    for (int j = 0; j < 4; ++j) {
      s0 = dot2bf(xr0[j], 0x3f803f80u, s0); q0 = dot2bf(xr0[j], xr0[j], q0);
      s1 = dot2bf(xr1[j], 0x3f803f80u, s1); q1 = dot2bf(xr1[j], xr1[j], q1);
    }
    auto ta = __builtin_amdgcn_permlane32_swap(f2u(s0), f2u(q0), false, false);
    auto tb = __builtin_amdgcn_permlane32_swap(f2u(s1), f2u(q1), false, false);
    auto w = __builtin_amdgcn_permlane16_swap(f2u(u2f(ta[0]) + u2f(ta[1])), f2u(u2f(tb[0]) + u2f(tb[1])), false, false);
    float r = u2f(w[0]) + u2f(w[1]);
    r = dpp_add<0x128>(r); r = dpp_add<0x124>(r); r = dpp_add<0x122>(r); r = dpp_add<0x121>(r);
    auto z = __builtin_amdgcn_permlane32_swap(f2u(r), f2u(r), false, false);
    float m = u2f(z[0]) * (1.f / D_), var = fmaxf(u2f(z[1]) * (1.f / D_) - m * m, 0.f);
    float sd = __builtin_amdgcn_sqrtf(rbf(rbf(var) + 1e-5f));
    const u32 so = (lane & 47) == 0 ? (((u32)WS_STG + (16 * P + (lane >> 1) + wv) * 8) | pmask) : OOB;
    bstore8_sc1(u32x2{pk_bf16(m, sd), tag}, rs_ws, so);
    __syncthreads();
    const u32* xt = (const u32*)(smem + A_XT);
#pragma unroll
    for (int i = 0; i < 4; ++i) { const int dp = 64 * i + (tid >> 3), tp = tid & 7; gx[i][0] = xt[(2 * tp) * 228 + dp]; gx[i][1] = xt[(2 * tp + 1) * 228 + dp]; }
    xgoff = ((tid >> 3) * 16384 + (tid & 7) * 16 + (u32)WS_XG + P * 128) | pmask;
#pragma unroll
    for (int i = 0; i < 4; ++i) if (i < XS0) xg_st(i);
    SB();
  }
  STAMPA(2)
  const u32 goff = wv * 2048 + lane * 16;
  const u32 mgoff = ((lane < 2 ? lane : 2) * D_ + d0) * 8;
  const int q = tid & 255;
  u32x4 sg0, sg1, xg0, xg1, mg, tb0, tb1;
  {
    if (PRIO_T > 0 && wv >= 4) __builtin_amdgcn_s_setprio(1);
    *(u32x4*)(smem + wv * TWAVE + lane * 16) = prm;
    __builtin_amdgcn_wave_barrier();
    const u16* pp = (const u16*)(smem + wv * TWAVE) + lane;
    float dt = rbf(expf(ch ? bfhi(ld2) : bflo(ld2)));
    float zr = dt * bf2f(pp[0]), zi = dt * bf2f(pp[64]);
    float dBr = dt * bf2f(pp[128]), dBi = dt * bf2f(pp[192]);
    float Cr = bf2f(pp[256]), Ci = bf2f(pp[320]);
    __builtin_amdgcn_wave_barrier();
    cf W = {Cr * dBr - Ci * dBi, Cr * dBi + Ci * dBr};
    float sw = wave_sum(fabsf(W[0]) + fabsf(W[1]));
    if (lane == 0) ((float*)(smem + A_SW))[wv] = sw;
    W *= pow2i(14 - exp_bound(sw));
    SB();
    if (XLO ? XLO0 + 0 < 7 : XS0 <= 1) xg_st(1);
    SB();
    char* rowp = smem + wv * TWAVE + lane * TROW;
    cf M1 = cexp_l(zr, zi, 1.f, 1.f), M8 = cexp_l(zr, zi, 8.f, -1.f), M128 = cexp_l(zr, zi, 128.f, 1.f);
    auto wr8 = [&](const cf (&e)[8], int offr, int offi) {
      *(u32x4*)(rowp + offr) = u32x4{pk_f16(e[0][0], e[1][0]), pk_f16(e[2][0], e[3][0]), pk_f16(e[4][0], e[5][0]), pk_f16(e[6][0], e[7][0])};
      *(u32x4*)(rowp + offi) = u32x4{pk_f16(e[0][1], e[1][1]), pk_f16(e[2][1], e[3][1]), pk_f16(e[4][1], e[5][1]), pk_f16(e[6][1], e[7][1])};
    };
    SB();
    if (XLO ? XLO0 + 1 < 7 : XS0 <= 2) xg_st(2);
    SB();
    cf Fa[8], Fb[8], M[8], R[8], G = W;
    Fa[0] = cf{1.f, 0.f}; Fa[1] = M8; Fa[2] = cmul(M8, M8); M[1] = M1; M[2] = cmul(M1, M1);
    Fa[3] = cmul(Fa[2], M8); Fa[4] = cmul(Fa[2], Fa[2]); M[3] = cmul(M[2], M1); M[4] = cmul(M[2], M[2]);
    cmul3(Fa[4], M8, Fa[2], Fa[3], Fa[5], Fa[6], Fa[7]);
    cmul3(M[4], M1, M[2], M[3], M[5], M[6], M[7]);
    Fb[0] = cmul(Fa[4], Fa[4]);
    wr8(Fa, 192, 224);
    if (PRIO_T == 1) __builtin_amdgcn_s_setprio(0);
    SB();
    if (XLO ? XLO0 + 2 < 7 : XS0 <= 3) xg_st(3);
    SB();
    if (PACKER) pack_load2(wgate, wup, wdown, cw * 4 + (wv & 3), 0u, lane, pv);
    SB();
    cmul4(Fb[0], Fa[1], Fa[2], Fa[3], Fa[4], Fb[1], Fb[2], Fb[3], Fb[4]);
    cmul3(Fb[0], Fa[5], Fa[6], Fa[7], Fb[5], Fb[6], Fb[7]);
    wr8(Fb, 208, 240);
#pragma unroll
    for (int A = 0; A < 2; ++A) {
      R[0] = G;
      cmul4(G, M[1], M[2], M[3], M128, R[1], R[2], R[3], G);
      cmul4(R[0], M[4], M[5], M[6], M[7], R[4], R[5], R[6], R[7]);
      wr8(R, 16 * A, 96 + 16 * A);
    }
    SB();
    if (PRIO_T == 2) __builtin_amdgcn_s_setprio(0);
    tb0 = *(const u32x4*)(idx_tab.v + q * 8); tb1 = *(const u32x4*)(idx_tab.v + q * 8 + 4);
    SB();
    R[0] = G; cmul4(R[0], M[2], M[4], M[6], M128, R[1], R[2], R[3], G);
    SB();
    xg0 = bload_sc1(rs_ws, goff, (u32)WS_XG + cw * 16384);
    SB();
    R[4] = G; cmul4(R[4], M[2], M[4], M[6], M128, R[5], R[6], R[7], G);
    wr8(R, 32, 128);
    if (PRIO_T == 3) __builtin_amdgcn_s_setprio(0);
    SB();
    xg1 = bload_sc1(rs_ws, goff, (u32)WS_XG + cw * 16384 + 1024);
    SB();
    {
      cf M256 = cmul(M128, M128);
      R[0] = G; R[2] = cmul(G, M128);
      cmul3(M256, R[0], R[2], R[0], R[4], R[6], G);
      cmul4(M[4], R[0], R[2], R[4], R[6], R[1], R[3], R[5], R[7]);
      G = cmul(R[4], M256);
      wr8(R, 48, 144);
      if (PRIO_T == 4) __builtin_amdgcn_s_setprio(0);
      SB();
      sg0 = bload_sc1(rs_ws, goff, (u32)WS_STG);
      SB();
      cf M512 = cmul(M256, M256);
      R[0] = G; R[1] = cmul(G, M128);
      { cf a, b2; cmul3(M256, R[0], R[1], R[0], R[2], R[3], a); (void)a; (void)b2; }
      cmul4(M512, R[0], R[1], R[2], R[3], R[4], R[5], R[6], R[7]);
      G = cmul(R[4], M512);
      wr8(R, 64, 160);
      SB();
      sg1 = bload_sc1(rs_ws, goff, (u32)WS_STG + 1024);
      mg = bload_sc1(rs_ws, mgoff, (u32)WS_MODG);
      SB();
      *(u32*)(rowp + 80) = pk_f16(G[0], 0.f); *(u32*)(rowp + 176) = pk_f16(G[1], 0.f);
    }
  }
  if (PRIO_T == 5) __builtin_amdgcn_s_setprio(0);
  __builtin_amdgcn_wave_barrier();
  STAMPA(4)
  {
    f32x4 acc[3] = {};
    const int g = lane >> 4, qq = (lane & 15) >> 2, p = lane & 3;
    const char* wb = smem + wv * TWAVE + (8 * (g & 1) + 2 * qq + (g >> 1)) * TROW + 8 * p;
#pragma unroll
    for (int pl = 0; pl < 2; ++pl)
#pragma unroll
      for (int s = 0; s < 2; ++s) {
        const char* r0 = wb + 32 * s * TROW;
        s16x8 bb = cat8(tr_read(r0 + 192 + 32 * pl), tr_read(r0 + 16 * TROW + 192 + 32 * pl));
#pragma unroll
        for (int t = 0; t < 3; ++t) {
          s16x8 aa = cat8(tr_read(r0 + 96 * pl + 32 * t), tr_read(r0 + 16 * TROW + 96 * pl + 32 * t));
          acc[t] = __builtin_amdgcn_mfma_f32_16x16x32_f16(__builtin_bit_cast(f16x8, aa), __builtin_bit_cast(f16x8, bb), acc[t], 0, 0, 0);
        }
      }
#pragma unroll
    for (int t = 0; t < 3; ++t) *(f32x4*)(smem + wv * TWAVE + t * 1024 + lane * 16) = acc[t];
  }
  STAMPA(5)
  auto do_pack = [&]() {
    if (PACKER) {
      pack_wr(smem + wv * TWAVE + 4096, lane, pv);
      pack_rd(smem + wv * TWAVE + 4096, cw * 4 + (wv & 3), lane, pv);
    }
  };
  do_pack();
  SB();
  pack_st(rs_ws, cw * 4 + (wv & 3), PACKER ? 0u : OOB, lane, pv);
  SB();
  const u32 tg = tag;
  if (!(__all(xg0[1] == tg && xg0[3] == tg && xg1[1] == tg && xg1[3] == tg) && __all(sg0[1] == tg && sg0[3] == tg && sg1[1] == tg && sg1[3] == tg))) {
    bool okx = false, oks = false;
#pragma nounroll
    for (u32 spins = 0; spins < SPIN_CAP; ++spins) {
      if (!okx) { xg0 = bload_sc1(rs_ws, goff, (u32)WS_XG + cw * 16384); xg1 = bload_sc1(rs_ws, goff, (u32)WS_XG + cw * 16384 + 1024); }
      if (!oks) { sg0 = bload_sc1(rs_ws, goff, (u32)WS_STG); sg1 = bload_sc1(rs_ws, goff, (u32)WS_STG + 1024); }
      okx = __all(xg0[1] == tg && xg0[3] == tg && xg1[1] == tg && xg1[3] == tg);
      oks = __all(sg0[1] == tg && sg0[3] == tg && sg1[1] == tg && sg1[3] == tg);
      if (okx && oks) break;
      __builtin_amdgcn_s_sleep(1);
    }
  }
  KEEP(pf);
  STAMPA(3)
  xv[0] = xg0[0]; xv[1] = xg0[2]; xv[2] = xg1[0]; xv[3] = xg1[2];
  float nn[4][2];
  {
    const u32 sv[4] = {sg0[0], sg0[2], sg1[0], sg1[2]};
    float nm[4], rs[4];
#pragma unroll
    for (int i = 0; i < 4; ++i) { nm[i] = -bflo(sv[i]); rs[i] = __builtin_amdgcn_rcpf(bfhi(sv[i])); nn[i][0] = bflo(xv[i]); nn[i][1] = bfhi(xv[i]); }
    SB();
#pragma unroll
    for (int i = 0; i < 4; ++i) { nn[i][0] += nm[i]; nn[i][1] += nm[i]; }
    SB();
#pragma unroll
    for (int i = 0; i < 4; ++i) { nn[i][0] = rbf(nn[i][0]); nn[i][1] = rbf(nn[i][1]); }
    SB();
#pragma unroll
    for (int i = 0; i < 4; ++i) { nn[i][0] *= rs[i]; nn[i][1] *= rs[i]; }
    SB();
#pragma unroll
    for (int i = 0; i < 4; ++i) { nn[i][0] = rbf(nn[i][0]); nn[i][1] = rbf(nn[i][1]); }
    asm volatile("" : "+v"(nn[0][0]), "+v"(nn[0][1]), "+v"(nn[1][0]), "+v"(nn[1][1]), "+v"(nn[2][0]), "+v"(nn[2][1]), "+v"(nn[3][0]), "+v"(nn[3][1]));
    SB();
  }
  auto pub_flags = [&]() {
    if (tid == 0) __hip_atomic_store((gu32*)(ws + WS_PKF + cw * PKS), tag, RLX_AGENT);
  };
  VM0();
  LBAR();
  pub_flags();
  bstore_a<16>(u32x4{X1_VOID, X1_VOID, X1_VOID, X1_VOID}, rs_ws, x1off);
  STAMPA(6) STAMPE(2)
  {
    char* kd = smem + A_KD + ch * 3072;
    f32x4 sw4 = *(const f32x4*)(smem + A_SW + ch * 16);
    f32x4 pp[4];
#pragma unroll
    for (int w = 0; w < 4; ++w) pp[w] = *(const f32x4*)(smem + (4 * ch + w) * TWAVE + q * 16);
    SB();
    asm volatile("" : "+v"(sw4));
    int exk = exp_bound(sw4[0] + sw4[1] + sw4[2] + sw4[3]);
    float scw[4];
#pragma unroll
    for (int w = 0; w < 4; ++w) scw[w] = pow2i(exp_bound(sw4[w]) - exk);
    SB();
    if (q < 192) {
      f32x4 s = {0.f, 0.f, 0.f, 0.f};
      s = (pp[0] * scw[0] + pp[1] * scw[1]) + (pp[2] * scw[2] + pp[3] * scw[3]);
      u32 k01 = pk_f16(s[0], s[1]), k23 = pk_f16(s[2], s[3]);
      *(u16*)(kd + (tb1[2] & 0xffffu)) = (u16)k01; *(u16*)(kd + (tb1[2] >> 16)) = (u16)(k01 >> 16);
      *(u16*)(kd + (tb1[3] & 0xffffu)) = (u16)k23; *(u16*)(kd + (tb1[3] >> 16)) = (u16)(k23 >> 16);
    } else if (q == 255) { *(u16*)(kd + KZERO) = 0; ((float*)(smem + A_SW + 32))[ch] = pow2i(exk - 14); }
  }
  STAMPE(3)
  if (tid >= 192 && tid < 256) *(u32x4*)(smem + A_H + ((tid >> 5) & 1) * 4608 + (tid & 31) * 16) = u32x4{0, 0, 0, 0};
  if (!__all(mg[1] == tg && mg[3] == tg)) {
#pragma nounroll
    for (u32 spins = 0; spins < SPIN_CAP; ++spins) {
      mg = bload_sc1(rs_ws, mgoff, (u32)WS_MODG);
      if (__all(mg[1] == tg && mg[3] == tg)) break;
      __builtin_amdgcn_s_sleep(1);
    }
  }
  STAMPA(7) STAMPE(4)
  {
    float sa[2], c1[2], tau[2], hh_[4][2];
#pragma unroll
    for (int c = 0; c < 2; ++c) {
      sa[c] = u2f(__builtin_amdgcn_readlane(mg[2 * c], 0)); c1[c] = u2f(__builtin_amdgcn_readlane(mg[2 * c], 1));
      ga[c] = u2f(__builtin_amdgcn_readlane(mg[2 * c], 2));
    }
#pragma unroll
    for (int i = 0; i < 4; ++i) { hh_[i][0] = nn[i][0] * c1[0]; hh_[i][1] = nn[i][1] * c1[1]; }
#pragma unroll
    for (int c = 0; c < 2; ++c) {
      const int ex = exp_bound(21.25f * 1.02f * fabsf(c1[c]) + 1.02f * fabsf(sa[c]));
      tau[c] = pow2i(15 - ex); post[c] = pow2i(ex - 15);
    }
    SB();
#pragma unroll
    for (int i = 0; i < 4; ++i) { hh_[i][0] = rbf(hh_[i][0]); hh_[i][1] = rbf(hh_[i][1]); }
    SB();
#pragma unroll
    for (int i = 0; i < 4; ++i) { hh_[i][0] += sa[0]; hh_[i][1] += sa[1]; }
    SB();
#pragma unroll
    for (int i = 0; i < 4; ++i) { hh_[i][0] = rbf(hh_[i][0]); hh_[i][1] = rbf(hh_[i][1]); }
    SB();
    float hs[2][4];
#pragma unroll
    for (int i = 0; i < 4; ++i) { hs[0][i] = hh_[i][0] * tau[0]; hs[1][i] = hh_[i][1] * tau[1]; hh_[i][0] *= bflo(ds2); hh_[i][1] *= bfhi(ds2); }
    SB();
#pragma unroll
    for (int c = 0; c < 2; ++c) *(u32x2*)(smem + A_H + c * 4608 + (256 + t0) * 2) = u32x2{pk_f16(hs[c][0], hs[c][1]), pk_f16(hs[c][2], hs[c][3])};
#pragma unroll
    for (int i = 0; i < 4; ++i) hdp[i] = pk_bf16(hh_[i][0], hh_[i][1]);
    float hd[4][2];
#pragma unroll
    for (int c = 0; c < 0; ++c) {
      float sa = u2f(__builtin_amdgcn_readlane(mg[2 * c], 0)), c1 = u2f(__builtin_amdgcn_readlane(mg[2 * c], 1));
      ga[c] = u2f(__builtin_amdgcn_readlane(mg[2 * c], 2));
      int ex = exp_bound(21.25f * 1.02f * fabsf(c1) + 1.02f * fabsf(sa));
      float tau = pow2i(15 - ex); post[c] = pow2i(ex - 15);
      float dk = c ? bfhi(ds2) : bflo(ds2);
#pragma unroll
      for (int i = 0; i < 4; ++i) {
        float h = rbf(rbf(nn[i][c] * c1) + sa);
        hd[i][c] = rbf(h * dk);
        hs[c][i] = h * tau;
      }
      *(u32x2*)(smem + A_H + c * 4608 + (256 + t0) * 2) = u32x2{pk_f16(hs[c][0], hs[c][1]), pk_f16(hs[c][2], hs[c][3])};
    }
#if !(LVL & 4)
#pragma unroll
    for (int i = 0; i < 4; ++i) hdp[i] = pk_bf16(hd[i][0], hd[i][1]);
#endif
    if (wv >= 4) {
      *(u32x4*)(smem + A_XS + (tid - 256) * 16) = u32x4{xv[0], xv[1], xv[2], xv[3]};
      *(u32x4*)(smem + A_XS + 4096 + (tid - 256) * 16) = u32x4{hdp[0], hdp[1], hdp[2], hdp[3]};
    }
  }
  STAMPE(5)
  LBAR();
  STAMPA(8)
  STAMPE(6)
  {
    const char* kd = smem + A_KD + ch * 3072;
    const u32 gi[6] = {tb0[0], tb0[1], tb0[2], tb0[3], tb1[0], tb1[1]};
    u32 e[11];
#pragma unroll
    for (int i = 0; i < 11; ++i) e[i] = *(const u16*)(kd + ((i & 1) ? (gi[i >> 1] >> 16) : (gi[i >> 1] & 0xffffu)));
    asm volatile("" :: "v"(e[0]), "v"(e[1]), "v"(e[2]), "v"(e[3]), "v"(e[4]), "v"(e[5]), "v"(e[6]), "v"(e[7]), "v"(e[8]), "v"(e[9]), "v"(e[10]));
    STAMPE(7)
    u32 ev[5], od[5];
#pragma unroll
    for (int i = 0; i < 5; ++i) { ev[i] = e[2 * i + 1] | (e[2 * i + 2] << 16); od[i] = e[2 * i] | (e[2 * i + 1] << 16); }
    char* base = smem + A_KC + ch * 4 * CS + 32 + q * 16;
    *(u32x4*)(base + 0 * CS) = u32x4{ev[1], ev[2], ev[3], ev[4]};
    *(u32x4*)(base + 1 * CS) = u32x4{od[1], od[2], od[3], od[4]};
    *(u32x4*)(base + 2 * CS) = u32x4{ev[0], ev[1], ev[2], ev[3]};
    *(u32x4*)(base + 3 * CS) = u32x4{od[0], od[1], od[2], od[3]};
    if (q < 8) *(u32x4*)(smem + A_KC + (ch * 4 + (q >> 1)) * CS + (q & 1) * 16) = u32x4{0, 0, 0, 0};
  }
  STAMPE(8)
  VM0();
  STAMPE(9)
  }
  constexpr int NB = 8 / G1A, MB = 32 / G1A;
  const int nt = (xcd % NB) * G1A + idx % G1A, mt = (xcd / NB) * MB + idx / G1A;
  const int mt2 = xcd * 4 + (idx >> 3), nt2 = idx & 7;
  const u32 wb1 = w1_base(nt, wv, 0), wb2 = w1_base(nt, wv, 1);
  const u32 wbl = w1_base(nt, wv & 3, 0);
  const u32 w1fo = (u32)WS_PKF + (7 * ((14 * nt >> 2) + ((lane >> 3) & 3)) + min(lane & 7, 6)) * PKS;
  const u32x4 ws_desc = {(u32)(u64)ws, (u32)((u64)ws >> 32) & 0xffffu, (u32)WS_END, 0x00020000u};
  u32x4 w[56];
  u32 w1t = 0, w1u = 0;
  u32 w1f0 = 0;
  if (W1P1 ? wv == 4 : wv >= 4) w1f0 = flag_load(rs_ws, w1fo);
  LDSP u32* const w1ok = (LDSP u32*)(smem + A_SW + 76);
  auto w1_seen = [&]() {
    while (__builtin_amdgcn_readfirstlane(__hip_atomic_load(w1ok, __ATOMIC_RELAXED, __HIP_MEMORY_SCOPE_WORKGROUP)) == 0u) __builtin_amdgcn_s_sleep(1);
    asm volatile("" ::: "memory");
  };
  LBAR();
  if (idx >= 4 && tid == 256) __hip_atomic_store((gu64*)(ws + WS_ARR) + blockIdx.x, ((u64)tag << 32) | tag, RLX_AGENT);
  STAMPA(9) STAMPC(2) STAMPE(10)
  auto tail = [&](auto HIc) {
    constexpr bool HI = HIc;
#pragma unroll
    for (int i = HI ? NHI0 : NDM; i < 28; ++i) w[i] = w1_load(rs_ws, wb1, wb2, lane, i);
    if (HI) asm volatile("s_waitcnt vmcnt(28)" ::: "memory");
    STAMPB(2) STAMPC(7)
    LBAR();
    STAMPC(8)
    phase_BC<HI>(smem, ws, out, rs_ws, tag, mt, nt, mt2, nt2, wv, lane, tid, w1t ^ w1u, w);
  };
  if (wv >= 4) {
    u32x4 arr0 = {}, arr1 = {};
    if (wv == 4) {
      flag_wait(rs_ws, w1fo, w1f0, tag);
      if (lane == 0) __hip_atomic_store(w1ok, 1u, __ATOMIC_RELAXED, __HIP_MEMORY_SCOPE_WORKGROUP);
    } else w1_seen();
    STAMPC(3)
#pragma unroll
    for (int i = 0; i < NDM; ++i) dma1k(ws_desc, smem + S_W + ((wv - 4) * NDM + i) * 1024, lane * 16, wbl + kstep(i) * 1024);
#pragma unroll
    for (int i = 0; i < NHI0; ++i) w[i] = w1_load(rs_ws, wb1, wb2, lane, i);
    if (wv == 4) {
      arr0 = bload_sc1(rs_ws, lane * 32, (u32)WS_ARR); arr1 = bload_sc1(rs_ws, lane * 32 + 16, (u32)WS_ARR);
#pragma nounroll
      for (u32 spins = 0; spins < SPIN_CAP; ++spins) {
        if (__builtin_expect(__all(arr0[0] == tag && arr0[2] == tag && arr1[0] == tag && arr1[2] == tag), 1)) break;
        __builtin_amdgcn_s_sleep(8);
        arr0 = bload_sc1(rs_ws, lane * 32, (u32)WS_ARR); arr1 = bload_sc1(rs_ws, lane * 32 + 16, (u32)WS_ARR);
      }
      if (blockIdx.x == 31 && lane == 0) __hip_atomic_store((gu32*)(ws + WS_EPOCH), tag - TAG_C + 1u, RLX_AGENT);
    }
    SB();
    STAMPC(4)
    tail(std::true_type{});
  } else {
    if (idx < 4) { if (W1P1) w1_seen(); else flag_wait(rs_ws, w1fo, flag_load(rs_ws, w1fo), tag); }
    else {
    u32 w1f = 0, nvoid = 0;
    const int cc = wv >> 1, kh = wv & 1;
    const int g = lane >> 4, b = lane & 15, r = (lane >> 2) & 3, c4 = lane & 3;
    const char* ab = smem + A_KC + (cc * 4 + r) * CS + 24 + 2 * (16 * (8 * kh + (g >> 1)) - 8 * (g & 1) + 4 * c4);
    const char* fb = smem + A_H + cc * 4608 + 2 * (256 + 16 * (b - 8 * kh - (g >> 1)) + 8 * (g & 1)) - 192;
    f32x4 acc[8] = {};
    s16x4 Al[4][8], Ah[4][8]; f16x8 Fm[4][8];
    auto rd = [&](int kk, int n) {
      int i = n / 3, k = n % 3;
      if (k == 0) Fm[kk][i] = *(const f16x8*)(fb + 192 - kk * 64 + i * 512);
      if (k == 1) Al[kk][i] = tr_read(ab + 8 + kk * 64 + i * 512);
      if (k == 2) Ah[kk][i] = tr_read(ab + kk * 64 + i * 512);
    };
#pragma unroll
    for (int n = 0; n < 24; ++n) rd(0, n);
    static_for<4>([&](auto KK) {
      constexpr int kk = KK;
      static_for<36>([&](auto N) {
        constexpr int n = N;
        constexpr int i = n < 8 ? 0 : n < 15 ? 1 : n < 21 ? 2 : n < 26 ? 3 : n < 30 ? 4 : n < 33 ? 5 : n < 35 ? 6 : 7;
        constexpr int st = i == 0 ? 0 : i == 1 ? 8 : i == 2 ? 15 : i == 3 ? 21 : i == 4 ? 26 : i == 5 ? 30 : i == 6 ? 33 : 35;
        constexpr int c = i + (n - st);
        acc[c] = __builtin_amdgcn_mfma_f32_16x16x32_f16(__builtin_bit_cast(f16x8, cat8(Al[kk][i], Ah[kk][i])), Fm[kk][c - i], acc[c], 0, 0, 0);
        if (kk < 3 && n >= 4 && n < 28) rd(kk < 3 ? kk + 1 : 0, n - 4);
        if (!W1P1 && kk == 2 && n == 0) w1f = flag_load(rs_ws, w1fo);
        SB();
      });
    });
#pragma unroll
    for (int c = 0; c < 8; ++c) *(f32x4*)(smem + A_RB + (wv * 8 + c) * 1024 + lane * 16) = acc[c];
    {
      LDSP u32* cnt = (LDSP u32*)(smem + A_SW + 68);
      asm volatile("s_waitcnt lgkmcnt(0)" ::: "memory");
      if (lane == 0) __hip_atomic_fetch_add(cnt, 1u, __ATOMIC_RELAXED, __HIP_MEMORY_SCOPE_WORKGROUP);
      while (__builtin_amdgcn_readfirstlane(__hip_atomic_load(cnt, __ATOMIC_RELAXED, __HIP_MEMORY_SCOPE_WORKGROUP)) != 4u) {}
      asm volatile("" ::: "memory");
    }
    STAMPA(10) STAMPC(5)
    const float* exs = (const float*)(smem + A_SW + 32);
    const float sc0 = exs[0] * post[0], sc1 = exs[1] * post[1];
    {
      f32x4 ya[2][2], yb[2][2];
#pragma unroll
      for (int hf = 0; hf < 2; ++hf)
#pragma unroll
        for (int c = 0; c < 2; ++c) {
          ya[hf][c] = *(const f32x4*)(smem + A_RB + ((2 * c) * 8 + wv + 4 * hf) * 1024 + lane * 16);
          yb[hf][c] = *(const f32x4*)(smem + A_RB + ((2 * c + 1) * 8 + wv + 4 * hf) * 1024 + lane * 16);
        }
      u32x4 xs[2], hq[2];
      xs[0] = u32x4{xv[0], xv[1], xv[2], xv[3]}; hq[0] = u32x4{hdp[0], hdp[1], hdp[2], hdp[3]};
      xs[1] = *(const u32x4*)(smem + A_XS + tid * 16); hq[1] = *(const u32x4*)(smem + A_XS + 4096 + tid * 16);
      SB();
      auto lvl = [&](auto f) {
#pragma unroll
        for (int hf = 0; hf < 2; ++hf)
#pragma unroll
          for (int c = 0; c < 2; ++c)
#pragma unroll
            for (int i = 0; i < 4; ++i) f(hf, c, i);
        SB();
      };
#pragma unroll
      for (int hf = 0; hf < 2; ++hf)
#pragma unroll
        for (int c = 0; c < 2; ++c) ya[hf][c] = (ya[hf][c] + yb[hf][c]) * (c ? sc1 : sc0);
      SB();
      lvl([&](int hf, int c, int i) { ya[hf][c][i] = rbf(ya[hf][c][i]); yb[hf][c][i] = c ? bfhi(hq[hf][i]) : bflo(hq[hf][i]); });
#pragma unroll
      for (int hf = 0; hf < 2; ++hf)
#pragma unroll
        for (int c = 0; c < 2; ++c) ya[hf][c] += yb[hf][c];
      SB();
      lvl([&](int hf, int c, int i) { ya[hf][c][i] = rbf(ya[hf][c][i]); yb[hf][c][i] = c ? bfhi(xs[hf][i]) : bflo(xs[hf][i]); });
#pragma unroll
      for (int hf = 0; hf < 2; ++hf)
#pragma unroll
        for (int c = 0; c < 2; ++c) ya[hf][c] *= ga[c];
      SB();
      lvl([&](int hf, int c, int i) { ya[hf][c][i] = rbf(ya[hf][c][i]); });
#pragma unroll
      for (int hf = 0; hf < 2; ++hf)
#pragma unroll
        for (int c = 0; c < 2; ++c) ya[hf][c] += yb[hf][c];
      SB();
#pragma unroll
      for (int hf = 0; hf < 2; ++hf) {
        u32x4 o;
#pragma unroll
        for (int i = 0; i < 4; ++i) o[i] = umn(pk_bf16(ya[hf][0][i], ya[hf][1][i]), X1_VOID - 1);
        bstore_a<16>(o, rs_ws, x1off + hf * (16 * 224 * 256));
      }
    }
#pragma unroll
    for (int hf = 0; hf < 0; ++hf) {
      f32x4 y[2];
#pragma unroll
      for (int c = 0; c < 2; ++c)
        y[c] = (*(const f32x4*)(smem + A_RB + ((2 * c) * 8 + wv + 4 * hf) * 1024 + lane * 16) + *(const f32x4*)(smem + A_RB + ((2 * c + 1) * 8 + wv + 4 * hf) * 1024 + lane * 16)) * (c ? sc1 : sc0);
      u32x4 xs = {xv[0], xv[1], xv[2], xv[3]}, hq = {hdp[0], hdp[1], hdp[2], hdp[3]};
      if (hf) { xs = *(const u32x4*)(smem + A_XS + tid * 16); hq = *(const u32x4*)(smem + A_XS + 4096 + tid * 16); }
      u32x4 o;
#pragma unroll
      for (int i = 0; i < 4; ++i) {
        float r0 = bflo(xs[i]) + rbf(ga[0] * rbf(rbf(y[0][i]) + bflo(hq[i])));
        float r1 = bfhi(xs[i]) + rbf(ga[1] * rbf(rbf(y[1][i]) + bfhi(hq[i])));
        o[i] = umn(pk_bf16(r0, r1), X1_VOID - 1);
      }
      bstore_a<16>(o, rs_ws, x1off + hf * (16 * 224 * 256));
    }
    STAMPC(6)
    if (W1P1) w1_seen(); else flag_wait(rs_ws, w1fo, w1f, tag);
    }
    STAMPC(4)
    tail(std::false_type{});
  }
  STAMP_RT(11)
  STAMP_OUT(0, ws)
}

extern "C" void kernel_launch(void* const* d_in, const int* in_sizes, int n_in, void* d_out, int out_size, void* d_ws, size_t ws_size,
                              hipStream_t stream) {
  k_all<<<256, 512, 0, stream>>>((const u16*)d_in[0], (const u16*)d_in[1], (const u16*)d_in[2], (const u16*)d_in[3], (const u16*)d_in[4], (const u16*)d_in[5],
                                 (const u16*)d_in[6], (const u16*)d_in[7], (const u16*)d_in[8], (const u16*)d_in[13], (const u16*)d_in[14],
                                 (const u16*)d_in[10], (const u16*)d_in[11], (const u16*)d_in[12], (u16*)d_out, (char*)d_ws);
}
```
